```python
import torch

def _softmax(x, axis=-1):
    return torch.softmax(x, dim=axis)
def _gelu(x):
    return torch.nn.functional.gelu(x, approximate="tanh")
def _silu(x):
    return torch.nn.functional.silu(x)
def _sigmoid(x):
    return torch.sigmoid(x)
def _layernorm(x, axis=-1, eps=1e-5):
    m = x.mean(dim=axis, keepdim=True)
    v = x.var(dim=axis, keepdim=True, unbiased=False)
    return (x - m) / torch.sqrt(v + eps)
def _scatter_add(tgt, idx, src, axis=0):
    return tgt.clone().index_add_(axis, idx, src)
def _segment_sum(src, seg, n):
    out = torch.zeros((n,) + src.shape[1:], dtype=src.dtype)
    return out.index_add_(0, seg, src)
def _pad(x, pw):
    flat = tuple(v for pair in reversed(pw) for v in pair)
    return torch.nn.functional.pad(x, flat)

kernel_name = 'sequential_preLN__linear_attn_block__dense_activation_squared_relu__prompt'

def setup_inputs():

    _w = (lambda a: (lambda t: t.to(torch.bfloat16) if t.is_floating_point() else t)(torch.as_tensor(a)))
    def _A_setup():
        B, S, D = 1, 2048, 448
        return {
            "x": _w((lambda *s: torch.randn(*s, dtype=torch.bfloat16) * 0.05)(B, S, D)),
            "wqkv": _w((lambda *s: torch.randn(*s, dtype=torch.bfloat16) * 0.1)(D, 3 * D)),
            "wo": _w((lambda *s: torch.randn(*s, dtype=torch.bfloat16) * 0.05)(D, D)),
        }
    _A_in = _A_setup()

    def _B_setup():
        B, S, D, F = 1, 2048, 448, 1792
        return {
            "x": _w((lambda *s: torch.randn(*s, dtype=torch.bfloat16) * 0.05)(B, S, D)),
            "w1": _w((lambda *s: torch.randn(*s, dtype=torch.bfloat16) * 0.05)(D, F)),
            "b1": _w((lambda *s: torch.randn(*s, dtype=torch.bfloat16) * 0.2)(F)),
            "w2": _w((lambda *s: torch.randn(*s, dtype=torch.bfloat16) * 0.05)(F, D)),
            "b2": _w((lambda *s: torch.randn(*s, dtype=torch.bfloat16) * 0.8)(D)),
        }
    _B_in = _B_setup()

    return {
        "A_x": _A_in["x"],
        "A_wqkv": _A_in["wqkv"],
        "A_wo": _A_in["wo"],
        "B_x": _B_in["x"],
        "B_w1": _B_in["w1"],
        "B_b1": _B_in["b1"],
        "B_w2": _B_in["w2"],
        "B_b2": _B_in["b2"],
    }

def reference(A_x, A_wqkv, A_wo, B_x, B_w1, B_b1, B_w2, B_b2):
    def _A_ref(x, wqkv, wo):
        B, S, D = x.shape
        H = 16; Dh = D // H
        qkv = (lambda x, *d: x.permute(*d))((x @ wqkv).reshape(B, S, 3, H, Dh), 2, 0, 3, 1, 4)
        q, k, v = qkv[0], qkv[1], qkv[2]
        phi = lambda t: torch.where(t > 0, t + 1, torch.exp(t)) + 1e-6
        pq, pk = phi(q), phi(k)
        kv = torch.einsum("bhsd,bhse->bhde", pk, v)
        z = torch.einsum("bhsd->bhd", pk)
        num = torch.einsum("bhqd,bhde->bhqe", pq, kv)
        den = torch.einsum("bhqd,bhd->bhq", pq, z)[..., None] + 1e-6
        o = num / den
        return (lambda x, *d: x.permute(*d))(o, 0, 2, 1, 3).reshape(B, S, D) @ wo

    def _B_ref(x, w1, b1, w2, b2):
        h = torch.einsum("bsd,df->bsf", x, w1) + b1
        zero = torch.zeros_like(h)
        r = torch.where(h > zero, h, zero)
        h = r * r
        out = torch.einsum("bsf,fd->bsd", h, w2) + b2
        return out

    x = A_x
    h = x + _A_ref(_layernorm(x, axis=-1), A_wqkv, A_wo)
    h2 = h + _B_ref(_layernorm(h, axis=-1), B_w1, B_b1, B_w2, B_b2)
    return h2
```

```cpp
#include <hip/hip_runtime.h>
#include <hip/hip_bf16.h>
typedef unsigned short u16;
constexpr int S=2048, D=448, H=16, DH=28, F=1792;
__device__ __forceinline__ float b2f(u16 v){ return __uint_as_float(((unsigned)v)<<16); }
__device__ __forceinline__ u16 f2b(float f){ unsigned u=__float_as_uint(f); u += 0x7fffu + ((u>>16)&1u); return (u16)(u>>16); }

__global__ void k_ln(const u16* x, const float* xf, float* y){
  int r=blockIdx.x; int t=threadIdx.x; float v[7]; float s=0;
  for(int i=0;i<7;i++){ v[i]= x? b2f(x[r*D+t+64*i]) : xf[r*D+t+64*i]; s+=v[i]; }
  for(int o=32;o;o>>=1) s+=__shfl_xor(s,o);
  float m=s/D; float q=0; for(int i=0;i<7;i++){ float d=v[i]-m; q+=d*d; }
  for(int o=32;o;o>>=1) q+=__shfl_xor(q,o);
  float rs=rsqrtf(q/D+1e-5f);
  for(int i=0;i<7;i++) y[r*D+t+64*i]=b2f(f2b((v[i]-m)*rs));
}
__global__ void k_gemm(const float* A, const u16* W, float* C, int N, int K){
  int n=blockIdx.x*64+threadIdx.x; int m=blockIdx.y; if(n>=N) return;
  float acc=0; for(int k=0;k<K;k++) acc+=A[(size_t)m*K+k]*b2f(W[(size_t)k*N+n]);
  C[(size_t)m*N+n]=acc;
}
__device__ __forceinline__ float phi(float t){ return (t>0? t+1.f : __expf(t)) + 1e-6f; }
__global__ void k_kv(const float* qkv, float* kv, float* z){
  int h=blockIdx.x; int t=threadIdx.x; if(t>=DH*(DH+1)) return; int d=t/(DH+1), e=t%(DH+1);
  float acc=0; for(int s=0;s<S;s++){ float pk=phi(qkv[(size_t)s*3*D+D+h*DH+d]); float v= e<DH? qkv[(size_t)s*3*D+2*D+h*DH+e]:1.f; acc+=pk*v; }
  if(e<DH) kv[(h*DH+d)*DH+e]=acc; else z[h*DH+d]=acc;
}
__global__ void k_att(const float* qkv, const float* kv, const float* z, float* o){
  int s=blockIdx.x; int c=threadIdx.x; int h=c/DH, e=c%DH; float num=0, den=0;
  for(int d=0;d<DH;d++){ float pq=phi(qkv[(size_t)s*3*D+h*DH+d]); num+=pq*kv[(h*DH+d)*DH+e]; den+=pq*z[h*DH+d]; }
  o[(size_t)s*D+c]=b2f(f2b(num/(den+1e-6f)));
}
__global__ void k_res(const u16* x, const float* a, float* h){ int i=blockIdx.x*256+threadIdx.x; h[i]=b2f(f2b(b2f(x[i])+a[i])); }
__global__ void k_act(float* h1, const u16* b1){ int i=blockIdx.x*256+threadIdx.x; float v=h1[i]+b2f(b1[i%F]); v=v>0?v:0; h1[i]=b2f(f2b(v*v)); }
__global__ void k_out(const float* h, const float* f, const u16* b2, u16* out){ int i=blockIdx.x*256+threadIdx.x; out[i]=f2b(h[i]+f[i]+b2f(b2[i%D])); }

extern "C" void kernel_launch(void* const* d_in, const int* in_sizes, int n_in, void* d_out, int out_size, void* d_ws, size_t ws_size, hipStream_t stream) {
  const u16* x=(const u16*)d_in[0]; const u16* wqkv=(const u16*)d_in[1]; const u16* wo=(const u16*)d_in[2];
  const u16* w1=(const u16*)d_in[4]; const u16* b1=(const u16*)d_in[5]; const u16* w2=(const u16*)d_in[6]; const u16* b2=(const u16*)d_in[7];
  float* ws=(float*)d_ws; float* xn=ws; float* qkv=xn+S*D; float* kv=qkv+S*3*D; float* z=kv+H*DH*DH; float* o=z+H*DH+64; float* a=o+S*D; float* h=a+S*D; float* hn=h+S*D; float* h1=hn+S*D; float* f=h1+S*F;
  k_ln<<<S,64,0,stream>>>(x,nullptr,xn);
  k_gemm<<<dim3(3*D/64,S),64,0,stream>>>(xn,wqkv,qkv,3*D,D);
  k_kv<<<H,832,0,stream>>>(qkv,kv,z);
  k_att<<<S,D,0,stream>>>(qkv,kv,z,o);
  k_gemm<<<dim3(D/64,S),64,0,stream>>>(o,wo,a,D,D);
  k_res<<<S*D/256,256,0,stream>>>(x,a,h);
  k_ln<<<S,64,0,stream>>>(nullptr,h,hn);
  k_gemm<<<dim3(F/64,S),64,0,stream>>>(hn,w1,h1,F,D);
  k_act<<<S*F/256,256,0,stream>>>(h1,b1);
  k_gemm<<<dim3(D/64,S),64,0,stream>>>(h1,w2,f,D,F);
  k_out<<<S*D/256,256,0,stream>>>(h,f,b2,(u16*)d_out);
}
```

```cpp
#include <hip/hip_runtime.h>
#include <hip/hip_bf16.h>
typedef unsigned short u16;
typedef unsigned u32;
typedef short s16x4 __attribute__((ext_vector_type(4)));
typedef short s16x2 __attribute__((ext_vector_type(2)));
typedef short s16x8 __attribute__((ext_vector_type(8)));
typedef __bf16 bf16x8 __attribute__((ext_vector_type(8)));
typedef __bf16 bf16x2 __attribute__((ext_vector_type(2)));
typedef float f32x16 __attribute__((ext_vector_type(16)));
typedef float f32x4 __attribute__((ext_vector_type(4)));
typedef float f32x2 __attribute__((ext_vector_type(2)));
typedef unsigned u32x4 __attribute__((ext_vector_type(4)));
typedef unsigned u32x2 __attribute__((ext_vector_type(2)));
#define LAS __attribute__((address_space(3)))
#define DI __device__ __forceinline__
typedef __amdgpu_buffer_rsrc_t rsrc_t;

constexpr int S=2048, D=448, F=1792;
constexpr u32 WS_PQ   = 0;
constexpr u32 WS_KVP  = WS_PQ + 64*32*1024;
constexpr u32 WS_KVT  = WS_KVP + 16*2*64*1024;
constexpr u32 WS_AUG  = WS_KVT + 32*1024;
constexpr u32 WS_H    = WS_AUG + 64*1024;
constexpr u32 WS_ST   = WS_H + 64*28*1024;
constexpr u32 WS_H1   = WS_ST + 64*14*256;
constexpr u32 WS_END  = WS_H1 + 64*112*1024;

DI rsrc_t mk(const void* p, u32 n){ return __builtin_amdgcn_make_buffer_rsrc((void*)p, 0, n, 0x00020000); }
DI u32x4 ld16(rsrc_t r, u32 v, u32 s){ return __builtin_amdgcn_raw_buffer_load_b128(r, v, s, 0); }
DI u32x2 ld8(rsrc_t r, u32 v, u32 s){ return __builtin_amdgcn_raw_buffer_load_b64(r, v, s, 0); }
DI void st16(u32x4 d, rsrc_t r, u32 v, u32 s){ __builtin_amdgcn_raw_buffer_store_b128(d, r, v, s, 0); }
DI void st8(u32x2 d, rsrc_t r, u32 v, u32 s){ __builtin_amdgcn_raw_buffer_store_b64(d, r, v, s, 0); }
DI void glds(rsrc_t r, char LAS* dst, u32 v, u32 s){ __builtin_amdgcn_raw_ptr_buffer_load_lds(r, (LAS void*)dst, 16, v, s, 0, 0); }
DI f32x16 mfma(u32x4 a, u32x4 b, f32x16 c){ return __builtin_amdgcn_mfma_f32_32x32x16_bf16(__builtin_bit_cast(bf16x8,a), __builtin_bit_cast(bf16x8,b), c, 0,0,0); }
DI u32x4 trfrag(const char LAS* p0, const char LAS* p1){
  s16x4 a=__builtin_amdgcn_ds_read_tr16_b64_v4i16((LAS s16x4*)p0);
  s16x4 b=__builtin_amdgcn_ds_read_tr16_b64_v4i16((LAS s16x4*)p1);
  return __builtin_bit_cast(u32x4, __builtin_shufflevector(a,b,0,1,2,3,4,5,6,7));
}
DI u32 pk(float a, float b){ f32x2 v={a,b}; return __builtin_bit_cast(u32, __builtin_convertvector(v, bf16x2)); }
DI float lo(u32 v){ return __uint_as_float(v<<16); }
DI float hi(u32 v){ return __uint_as_float(v&0xffff0000u); }
DI u32x4 pack8(const f32x16& a, int s){ u32x4 r={pk(a[8*s],a[8*s+1]),pk(a[8*s+2],a[8*s+3]),pk(a[8*s+4],a[8*s+5]),pk(a[8*s+6],a[8*s+7])}; return r; }
DI float phi(float t){ return fmaxf(t,0.f) + __builtin_amdgcn_exp2f(fminf(t*1.44269504089f,0.f)) + 1e-6f; }
DI float xhalf_sum(float v){ auto r=__builtin_amdgcn_permlane32_swap(__float_as_uint(v),__float_as_uint(v),false,false); return __uint_as_float(r[0])+__uint_as_float(r[1]); }
DI float bcast_hi(float v){ auto r=__builtin_amdgcn_permlane32_swap(__float_as_uint(v),__float_as_uint(v),false,false); return __uint_as_float(r[1]); }
#define BAR()  asm volatile("s_barrier" ::: "memory")
#define BARL() asm volatile("s_waitcnt lgkmcnt(0)\n\ts_barrier" ::: "memory")
#define WAITV(n) asm volatile("s_waitcnt vmcnt(" #n ")" ::: "memory")

constexpr int LDS_BYTES = 163840;
constexpr int A_WKV=0, A_X=100352;

__global__ void __launch_bounds__(512,2) k_A(const u16* x, const u16* wqkv, char* ws){
  extern __shared__ __attribute__((aligned(16))) char smem_[];
  char LAS* smem=(char LAS*)smem_;
  const int b=blockIdx.x, t=(b&7)*4+((b>>3)&3), g=b>>5;
  const int w=__builtin_amdgcn_readfirstlane(threadIdx.x>>6), l=threadIdx.x&63;
  const int rot=t%7;
  if(w>=4){
    const int lw=w-4;
    rsrc_t rw=mk(wqkv, D*3*D*2);
    u32 pk_[7], pq_[7];
    #pragma unroll
    for(int i=0;i<7;i++){ int c=i*64+l, blk=c/112, rem=c%112, q=rem/28, r28=rem%28;
      pk_[i]=(8*blk+4*(r28/14)+q)*2688 + (1+(r28%14)/7)*896 + (r28%7)*16;
      pq_[i]=(16*blk+4*(r28/7)+q)*2688 + (r28%7)*16; }
    auto kvper=[&](int p){ int ch=(p>>1)+rot; if(ch>=7) ch-=7; u32 so=(ch*64+(p&1)*32)*2688+g*112;
      #pragma unroll
      for(int i=0;i<7;i++) glds(rw, smem+A_WKV+p*7168+i*1024, pk_[i], so); };
    auto qper=[&](int p){ int ch=p+rot; if(ch>=7) ch-=7; u32 so=ch*64*2688+g*112;
      #pragma unroll
      for(int i=0;i<7;i++) glds(rw, smem+A_X+p*7168+i*1024, pq_[i], so); };
    for(int r=0;r<3;r++) kvper(4*r+lw);
    WAITV(7); BAR();
    if(lw<2){ kvper(12+lw); WAITV(7); } else { WAITV(0); }
    BAR();
    if(lw<2){ WAITV(0); } else qper(lw-2);
    BAR();
    qper(2+lw); if(lw==0) qper(6);
    WAITV(0); BAR();
    return;
  }
  rsrc_t rx=mk(x, S*D*2), rs=mk(ws, WS_END);
  const int th=w>>1, hd=w&1, hh=l>>5, r=l&31;
  {
    const int row=16*w+(l>>2), q=l&3;
    u32x4 raw[14];
    #pragma unroll
    for(int i=0;i<14;i++){ int ch=(i>>1)+rot; if(ch>=7) ch-=7; raw[i]=ld16(rx, row*896+q*16+(i&1)*64, t*64*896+ch*128); }
    float v[112]; float s=0;
    #pragma unroll
    for(int i=0;i<14;i++){
      #pragma unroll
      for(int e=0;e<4;e++){ v[i*8+2*e]=lo(raw[i][e]); v[i*8+2*e+1]=hi(raw[i][e]); s+=v[i*8+2*e]+v[i*8+2*e+1]; } }
    s+=__shfl_xor(s,1); s+=__shfl_xor(s,2);
    const float m=s*(1.f/448); float qq=0;
    #pragma unroll
    for(int i=0;i<112;i++){ v[i]-=m; qq+=v[i]*v[i]; }
    qq+=__shfl_xor(qq,1); qq+=__shfl_xor(qq,2);
    const float rs_=__builtin_amdgcn_rsqf(qq*(1.f/448)+1e-5f);
    char LAS* xw=smem+A_X+(th*56+q)*512+(((row&31)^(2*q))*16);
    #pragma unroll
    for(int i=0;i<14;i++){ u32x4 o;
      #pragma unroll
      for(int e=0;e<4;e++) o[e]=pk(v[i*8+2*e]*rs_, v[i*8+2*e+1]*rs_);
      *(LAS u32x4*)(xw+i*2048)=o; }
  }
  BARL();
  u32x4 xf[28];
  { const char LAS* x0=smem+A_X+(th*56+hh)*512+((r^(2*hh))*16);
    const char LAS* x1=smem+A_X+(th*56+hh)*512+((r^(4+2*hh))*16)+1024;
    #pragma unroll
    for(int s=0;s<14;s++){ xf[2*s]=*(const LAS u32x4*)(x0+s*2048); xf[2*s+1]=*(const LAS u32x4*)(x1+s*2048); } }
  const int q=(l&15)>>2, p=l&3, cb=(l>>4)&1;
  const char LAS* wb=smem+A_WKV+hh*1792+q*448+hd*56+cb*32+p*8;
  f32x16 aK={}, aV={};
  #define KVSTEP(s) { const char LAS* a=wb+(s)*3584; aK=mfma(xf[s],trfrag(a,a+224),aK); aV=mfma(xf[s],trfrag(a+112,a+336),aV); }
  #pragma unroll
  for(int s=0;s<16;s++) KVSTEP(s)
  BARL();
  #pragma unroll
  for(int s=16;s<24;s++) KVSTEP(s)
  BAR();
  #pragma unroll
  for(int s=24;s<28;s++) KVSTEP(s)
  const int head=2*g+hd, tt=2*t+th;
  {
    #pragma unroll
    for(int i=0;i<16;i++){ aK[i]=phi(aK[i]); }
    u32x4 k0=pack8(aK,0), k1=pack8(aK,1), v0=pack8(aV,0), v1=pack8(aV,1);
    const u32 vp = r==28? 0x3f803f80u : 0u;
    #pragma unroll
    for(int e=0;e<4;e++){ if(r>=28){ k0[e]=0; k1[e]=0; v0[e]=vp; v1[e]=vp; } }
    f32x16 kv={}; kv=mfma(k0,v0,kv); kv=mfma(k1,v1,kv);
    st16(pack8(kv,0), rs, l*16, WS_KVP+((head*2+0)*64+tt)*1024);
    st16(pack8(kv,1), rs, l*16, WS_KVP+((head*2+1)*64+tt)*1024);
  }
  BAR();
  { const char LAS* qb=smem+A_X+q*448+2*hh*112+hd*56+cb*32+p*8;
    f32x16 aQ={};
    #pragma unroll
    for(int s=0;s<28;s++){ const char LAS* a=qb+s*1792; aQ=mfma(trfrag(a,a+112),xf[s],aQ); }
    #pragma unroll
    for(int i=0;i<16;i++) aQ[i]=phi(aQ[i]);
    u32x4 q0=pack8(aQ,0), q1=pack8(aQ,1);
    if(hh){ q1[2]=0; q1[3]=0; }
    st16(q0, rs, l*16, WS_PQ+((tt*16+head)*2+0)*1024);
    st16(q1, rs, l*16, WS_PQ+((tt*16+head)*2+1)*1024);
  }
}

__global__ void __launch_bounds__(256) k_R(const u16* w1, const u16* b1, char* ws){
  __shared__ float red[4*8*64];
  const int b=blockIdx.x, w=threadIdx.x>>6, l=threadIdx.x&63;
  rsrc_t rs=mk(ws, WS_END);
  if(b<32){
    u32x4 v[16];
    #pragma unroll
    for(int i=0;i<16;i++) v[i]=ld16(rs, l*16+(w*16+i)*1024, WS_KVP+b*65536);
    float a[8]={};
    #pragma unroll
    for(int i=0;i<16;i++){
      #pragma unroll
      for(int e=0;e<4;e++){ a[2*e]+=lo(v[i][e]); a[2*e+1]+=hi(v[i][e]); } }
    #pragma unroll
    for(int e=0;e<8;e++) red[(w*8+e)*64+l]=a[e];
    __syncthreads();
    if(w==0){
      #pragma unroll
      for(int e=0;e<8;e++) a[e]=red[e*64+l]+red[(8+e)*64+l]+red[(16+e)*64+l]+red[(24+e)*64+l];
      u32x4 o={pk(a[0],a[1]),pk(a[2],a[3]),pk(a[4],a[5]),pk(a[6],a[7])};
      st16(o, rs, l*16, WS_KVT+b*1024);
    }
  } else {
    const int c0=(b-32)*64, cp=threadIdx.x&7, rg=threadIdx.x>>3;
    rsrc_t rw=mk(w1, D*F*2);
    u32x4 v[14];
    #pragma unroll
    for(int i=0;i<14;i++) v[i]=ld16(rw, (rg+32*i)*3584+cp*16, c0*2);
    float a[8]={};
    #pragma unroll
    for(int i=0;i<14;i++){
      #pragma unroll
      for(int e=0;e<4;e++){ a[2*e]+=lo(v[i][e]); a[2*e+1]+=hi(v[i][e]); } }
    #pragma unroll
    for(int e=0;e<8;e++) red[rg*64+cp*8+e]=a[e];
    __syncthreads();
    if(w<2){
      const int r=l&31, hh=l>>5; float s=0;
      #pragma unroll
      for(int i=0;i<16;i++) s+=red[(hh*16+i)*64+w*32+r];
      s=xhalf_sum(s);
      u32 ch=pk(s,s); float rem=s-lo(ch); u32 cl=pk(rem,0.f);
      u32 bb=b1[c0+w*32+r]; bb|=bb<<16;
      u32x4 o={ch,cl,bb,0u}; if(hh) o=u32x4{0,0,0,0};
      st16(o, rs, l*16, WS_AUG+((b-32)*2+w)*1024);
    }
  }
}

DI u32 img_src(int l, int rowstride){ int row=l>>3, pos=l&7; return row*rowstride + ((pos^(((row>>1)&1)<<2))*16); }
DI int img_rd(int l, int nt){ int hh=l>>5, cb=(l>>4)&1, q=(l&15)>>2, p=l&3; return (4*hh+q)*128 + ((nt^(q>>1))*64) + cb*32 + p*8; }

__global__ void __launch_bounds__(512,2) k_E(const u16* x, const u16* wo, char* ws){
  extern __shared__ __attribute__((aligned(16))) char smem_[];
  char LAS* smem=(char LAS*)smem_;
  const int b=blockIdx.x, t=(b&7)*4+((b>>3)&3), j=b>>5;
  const int w=__builtin_amdgcn_readfirstlane(threadIdx.x>>6), l=threadIdx.x&63;
  constexpr int E_RED=57344+512;
  if(w>=4){
    const int lw=w-4; rsrc_t rw=mk(wo, D*D*2);
    const u32 src=img_src(l,896);
    for(int i=0;i<14;i++){ int blk=i*4+lw; glds(rw, smem+blk*1024, src, blk*8*896+j*128); }
    if(lw==0) *(LAS u32x2*)(smem+57344+l*8)=u32x2{0,0};
    WAITV(0); BARL(); BAR(); return;
  }
  rsrc_t rs=mk(ws, WS_END), rx=mk(x, S*D*2);
  const int th=w>>1, kh=w&1, hh=l>>5, r=l&31, tt=2*t+th;
  u32x4 pq[16], kv[16];
  #pragma unroll
  for(int i=0;i<16;i++){ kv[i]=ld16(rs, l*16+i*1024, WS_KVT+kh*16384); pq[i]=ld16(rs, l*16+i*1024, WS_PQ+tt*32768+kh*16384); }
  u32x2 xr[4];
  #pragma unroll
  for(int gq=0;gq<4;gq++) xr[gq]=ld8(rx, r*896+hh*8+gq*16, tt*32*896+j*128+kh*64);
  BAR();
  const char LAS* w0=smem+img_rd(l,0)+kh*8*28*128; const char LAS* w1_=smem+img_rd(l,1)+kh*8*28*128;
  f32x16 acc0={}, acc1={};
  #pragma unroll
  for(int h=0;h<8;h++){
    f32x16 num={}; num=mfma(kv[2*h],pq[2*h],num); num=mfma(kv[2*h+1],pq[2*h+1],num);
    const float rc=__builtin_amdgcn_rcpf(bcast_hi(num[12])+1e-6f);
    #pragma unroll
    for(int i=0;i<16;i++) num[i]*=rc;
    u32x4 o0=pack8(num,0), o1=pack8(num,1);
    if(hh){ o1[2]=0; o1[3]=0; }
    const int ro=h*28*128;
    acc0=mfma(trfrag(w0+ro,w0+ro+1024),o0,acc0); acc1=mfma(trfrag(w1_+ro,w1_+ro+1024),o0,acc1);
    acc0=mfma(trfrag(w0+ro+2048,w0+ro+3072),o1,acc0); acc1=mfma(trfrag(w1_+ro+2048,w1_+ro+3072),o1,acc1);
  }
  f32x16 keep = kh? acc1:acc0, send = kh? acc0:acc1;
  #pragma unroll
  for(int i=0;i<4;i++) *(LAS f32x4*)(smem+E_RED+w*4096+i*1024+l*16)=f32x4{send[4*i],send[4*i+1],send[4*i+2],send[4*i+3]};
  BARL();
  float s1=0,s2=0;
  #pragma unroll
  for(int i=0;i<4;i++){ f32x4 o=*(const LAS f32x4*)(smem+E_RED+(w^1)*4096+i*1024+l*16);
    float xv[4]={lo(xr[i][0]),hi(xr[i][0]),lo(xr[i][1]),hi(xr[i][1])};
    #pragma unroll
    for(int e=0;e<4;e++){ float v=keep[4*i+e]+o[e]+xv[e]; keep[4*i+e]=v; s1+=v; s2+=v*v; } }
  st16(pack8(keep,0), rs, l*16, WS_H+(tt*28+4*j+2*kh)*1024);
  st16(pack8(keep,1), rs, l*16, WS_H+(tt*28+4*j+2*kh+1)*1024);
  s1=xhalf_sum(s1); s2=xhalf_sum(s2);
  if(hh==0) st8(u32x2{__float_as_uint(s1),__float_as_uint(s2)}, rs, r*8, WS_ST+(tt*14+2*j+kh)*256);
}

DI void load_stats(rsrc_t rs, int tt, int l, float& mu, float& var){
  const int r=l&31, hh=l>>5; float s1=0,s2=0;
  u32x2 st[7];
  #pragma unroll
  for(int i=0;i<7;i++) st[i]=ld8(rs, r*8+hh*7*256+i*256, WS_ST+tt*14*256);
  #pragma unroll
  for(int i=0;i<7;i++){ s1+=__uint_as_float(st[i][0]); s2+=__uint_as_float(st[i][1]); }
  s1=xhalf_sum(s1); s2=xhalf_sum(s2);
  mu=s1*(1.f/448); var=fmaxf(s2*(1.f/448)-mu*mu,0.f)+1e-5f;
}

__global__ void __launch_bounds__(512,2) k_F(const u16* w1, char* ws){
  extern __shared__ __attribute__((aligned(16))) char smem_[];
  char LAS* smem=(char LAS*)smem_;
  const int b=blockIdx.x, t=(b&7)*4+((b>>3)&3), j=b>>5;
  const int w=__builtin_amdgcn_readfirstlane(threadIdx.x>>6), l=threadIdx.x&63;
  const int c=t>>1, fc=(t&1)*7+j;
  if(w>=4){
    const int lw=w-4; rsrc_t rw=mk(w1, D*F*2);
    const u32 src=img_src(l,3584);
    for(int hf=0;hf<2;hf++) for(int i=0;i<14;i++){ int blk=i*4+lw; glds(rw, smem+hf*57344+blk*1024, src, blk*8*3584+fc*256+hf*128);
      if(hf==1 && i==6){ WAITV(7); BAR(); } }
    WAITV(0); BAR(); return;
  }
  rsrc_t rs=mk(ws, WS_END);
  const int hh=l>>5, tt=4*c+w;
  float mu,var; load_stats(rs,tt,l,mu,var);
  u32x4 aug[4];
  #pragma unroll
  for(int i=0;i<4;i++) aug[i]=ld16(rs, l*16+i*1024, WS_AUG+fc*4096);
  u32x4 hf_[28];
  #pragma unroll
  for(int s=0;s<28;s++) hf_[s]=ld16(rs, l*16+(s&3)*1024, WS_H+(tt*28+(s&~3))*1024);
  u32x4 bs;
  { const float sg=__builtin_amdgcn_sqrtf(var); u32 mh=pk(-mu,-mu); float mr=-mu-lo(mh); u32 sh=pk(sg,sg); float sr=sg-lo(sh);
    bs=u32x4{pk(lo(mh),mr), mh&0xffffu, pk(lo(sh),sr), 0u}; if(hh) bs=u32x4{0,0,0,0}; }
  const char LAS* w0=smem+img_rd(l,0); const char LAS* w1_=smem+img_rd(l,1);
  for(int hf=0;hf<2;hf++){
    BAR();
    f32x16 a0={},a1={};
    a0=mfma(hf?aug[2]:aug[0],bs,a0); a1=mfma(hf?aug[3]:aug[1],bs,a1);
    #pragma unroll
    for(int s=0;s<28;s++){ const int ro=s*2048;
      a0=mfma(trfrag(w0+ro,w0+ro+1024),hf_[s],a0); a1=mfma(trfrag(w1_+ro,w1_+ro+1024),hf_[s],a1); }
    #pragma unroll
    for(int i=0;i<16;i++){ a0[i]=a0[i]*fmaxf(a0[i],0.f); a1[i]=a1[i]*fmaxf(a1[i],0.f); }
    const u32 so=WS_H1+(tt*112+fc*8+hf*4)*1024;
    st16(pack8(a0,0), rs, l*16, so); st16(pack8(a0,1), rs, l*16+1024, so);
    st16(pack8(a1,0), rs, l*16+2048, so); st16(pack8(a1,1), rs, l*16+3072, so);
    w0+=57344; w1_+=57344;
  }
}

__global__ void __launch_bounds__(512,2) k_G(const u16* w2, const u16* b2, char* ws, u16* out){
  extern __shared__ __attribute__((aligned(16))) char smem_[];
  char LAS* smem=(char LAS*)smem_;
  const int b=blockIdx.x, t=(b&7)*4+((b>>3)&3), j=b>>5;
  const int w=__builtin_amdgcn_readfirstlane(threadIdx.x>>6), l=threadIdx.x&63;
  constexpr int G_RED=131072;
  if(w>=4){
    const int lw=w-4; rsrc_t rw=mk(w2, D*F*2);
    const u32 src=img_src(l,896);
    auto stage=[&](int st){
      #pragma unroll
      for(int i=0;i<8;i++){ int blk=2*i+(lw>>1); glds(rw, smem+(st&3)*32768+(lw&1)*16384+blk*1024, src, ((lw&1)*896+st*128+blk*8)*896+j*128); } };
    stage(0); stage(1);
    for(int st=0;st<7;st++){
      if(st+2<7){ stage(st+2); WAITV(16); } else if(st+1<7){ WAITV(8); } else { WAITV(0); }
      BAR();
    }
    BAR(); return;
  }
  rsrc_t rs=mk(ws, WS_END), ro_=mk(out, S*D*2);
  const int th=w>>1, kh=w&1, hh=l>>5, r=l&31, tt=2*t+th;
  const u32 hb=WS_H1+(tt*112+kh*56)*1024;
  u32x4 a[3][8];
  #pragma unroll
  for(int i=0;i<8;i++) a[0][i]=ld16(rs, l*16+(i&3)*1024, hb+(i&~3)*1024);
  #pragma unroll
  for(int i=0;i<8;i++) a[1][i]=ld16(rs, l*16+(i&3)*1024, hb+(8+(i&~3))*1024);
  const char LAS* w0=smem+img_rd(l,0)+kh*16384; const char LAS* w1_=smem+img_rd(l,1)+kh*16384;
  f32x16 acc0={}, acc1={};
  #pragma unroll
  for(int st=0;st<7;st++){
    if(st+2<7){
      #pragma unroll
      for(int i=0;i<8;i++) a[(st+2)%3][i]=ld16(rs, l*16+(i&3)*1024, hb+((st+2)*8+(i&~3))*1024);
    }
    BARL();
    #pragma unroll
    for(int s=0;s<8;s++){ const int ro=(st&3)*32768+s*2048;
      acc0=mfma(trfrag(w0+ro,w0+ro+1024),a[st%3][s],acc0); acc1=mfma(trfrag(w1_+ro,w1_+ro+1024),a[st%3][s],acc1); }
  }
  float mu,var; load_stats(rs,tt,l,mu,var);
  u32x4 h0=ld16(rs, l*16, WS_H+(tt*28+4*j+2*kh)*1024), h1=ld16(rs, l*16+1024, WS_H+(tt*28+4*j+2*kh)*1024);
  rsrc_t rb=mk(b2, D*2);
  u32x2 bv[4];
  #pragma unroll
  for(int gq=0;gq<4;gq++) bv[gq]=ld8(rb, hh*8+gq*16, j*128+kh*64);
  f32x16 keep = kh? acc1:acc0, send = kh? acc0:acc1;
  #pragma unroll
  for(int i=0;i<4;i++) *(LAS f32x4*)(smem+G_RED+w*4096+i*1024+l*16)=f32x4{send[4*i],send[4*i+1],send[4*i+2],send[4*i+3]};
  BARL();
  const float rv=__builtin_amdgcn_rcpf(var);
  u32 hp[8]={h0[0],h0[1],h0[2],h0[3],h1[0],h1[1],h1[2],h1[3]};
  u32 o[8];
  #pragma unroll
  for(int i=0;i<4;i++){ f32x4 p=*(const LAS f32x4*)(smem+G_RED+(w^1)*4096+i*1024+l*16);
    float v0=(keep[4*i]+p[0])*rv+lo(bv[i][0])+lo(hp[2*i]), v1=(keep[4*i+1]+p[1])*rv+hi(bv[i][0])+hi(hp[2*i]);
    float v2=(keep[4*i+2]+p[2])*rv+lo(bv[i][1])+lo(hp[2*i+1]), v3=(keep[4*i+3]+p[3])*rv+hi(bv[i][1])+hi(hp[2*i+1]);
    o[2*i]=pk(v0,v1); o[2*i+1]=pk(v2,v3); }
  #pragma unroll
  for(int k=0;k<2;k++){
    auto r0=__builtin_amdgcn_permlane32_swap(o[4*k],o[4*k+2],false,false);
    auto r1=__builtin_amdgcn_permlane32_swap(o[4*k+1],o[4*k+3],false,false);
    st16(u32x4{r0[0],r1[0],r0[1],r1[1]}, ro_, r*896+hh*16+k*32, tt*32*896+j*128+kh*64);
  }
}

extern "C" void kernel_launch(void* const* d_in, const int* in_sizes, int n_in, void* d_out, int out_size, void* d_ws, size_t ws_size, hipStream_t stream) {
  const u16* x=(const u16*)d_in[0]; const u16* wqkv=(const u16*)d_in[1]; const u16* wo=(const u16*)d_in[2];
  const u16* w1=(const u16*)d_in[4]; const u16* b1=(const u16*)d_in[5]; const u16* w2=(const u16*)d_in[6]; const u16* b2=(const u16*)d_in[7];
  char* ws=(char*)d_ws;
  (void)hipFuncSetAttribute((const void*)k_A, hipFuncAttributeMaxDynamicSharedMemorySize, LDS_BYTES);
  (void)hipFuncSetAttribute((const void*)k_E, hipFuncAttributeMaxDynamicSharedMemorySize, LDS_BYTES);
  (void)hipFuncSetAttribute((const void*)k_F, hipFuncAttributeMaxDynamicSharedMemorySize, LDS_BYTES);
  (void)hipFuncSetAttribute((const void*)k_G, hipFuncAttributeMaxDynamicSharedMemorySize, LDS_BYTES);
  k_A<<<256,512,LDS_BYTES,stream>>>(x,wqkv,ws);
  k_R<<<60,256,0,stream>>>(w1,b1,ws);
  k_E<<<224,512,LDS_BYTES,stream>>>(x,wo,ws);
  k_F<<<224,512,LDS_BYTES,stream>>>(w1,ws);
  k_G<<<224,512,LDS_BYTES,stream>>>(w2,b2,ws,(u16*)d_out);
}
```

```cpp
#include <hip/hip_runtime.h>
#include <hip/hip_bf16.h>
typedef unsigned short u16;
typedef unsigned u32;
typedef unsigned long long u64;
typedef short s16x4 __attribute__((ext_vector_type(4)));
typedef __bf16 bf16x8 __attribute__((ext_vector_type(8)));
typedef __bf16 bf16x2 __attribute__((ext_vector_type(2)));
typedef float f32x16 __attribute__((ext_vector_type(16)));
typedef float f32x4 __attribute__((ext_vector_type(4)));
typedef float f32x2 __attribute__((ext_vector_type(2)));
typedef unsigned u32x4 __attribute__((ext_vector_type(4)));
typedef unsigned u32x2 __attribute__((ext_vector_type(2)));
#define LAS __attribute__((address_space(3)))
#define DI __device__ __forceinline__
typedef __amdgpu_buffer_rsrc_t rsrc_t;

constexpr int S=2048, D=448, F=1792;
constexpr u32 WS_EPOCH= 0;
constexpr u32 WS_IB1  = 1024;
constexpr u32 WS_IB2  = WS_IB1 + 8*256;
constexpr u32 WS_IB3  = WS_IB2 + 56*256;
constexpr u32 WS_IB4  = WS_IB3 + 64*256;
constexpr u32 WS_KVT  = 131072;
constexpr u32 WS_AUG  = WS_KVT + 32*1024;
constexpr u32 WS_ST   = WS_AUG + 64*1024;
constexpr u32 WS_PQ   = WS_ST + 64*14*256;
constexpr u32 WS_KVP  = WS_PQ + 64*32*1024;
constexpr u32 WS_H    = WS_KVP + 16*2*64*1024;
constexpr u32 WS_H1   = WS_H + 64*28*1024;
constexpr u32 WS_END  = WS_H1 + 64*112*1024;
static_assert(WS_IB4 + 3*64*256 <= WS_KVT, "inbox overflow");

constexpr int SC1=16;
DI rsrc_t mk(const void* p, u32 n){ return __builtin_amdgcn_make_buffer_rsrc((void*)p, 0, n, 0x00020000); }
DI u32x4 ld16(rsrc_t r, u32 v, u32 s){ return __builtin_amdgcn_raw_buffer_load_b128(r, v, s, 0); }
DI u32x2 ld8(rsrc_t r, u32 v, u32 s){ return __builtin_amdgcn_raw_buffer_load_b64(r, v, s, 0); }
DI u32x4 ld16c(rsrc_t r, u32 v, u32 s){ return __builtin_amdgcn_raw_buffer_load_b128(r, v, s, SC1); }
DI u32x2 ld8c(rsrc_t r, u32 v, u32 s){ return __builtin_amdgcn_raw_buffer_load_b64(r, v, s, SC1); }
DI void st16(u32x4 d, rsrc_t r, u32 v){ __builtin_amdgcn_raw_buffer_store_b128(d, r, v, 0, 0); }
DI void st16c(u32x4 d, rsrc_t r, u32 v){ __builtin_amdgcn_raw_buffer_store_b128(d, r, v, 0, SC1); }
DI void st8c(u32x2 d, rsrc_t r, u32 v){ __builtin_amdgcn_raw_buffer_store_b64(d, r, v, 0, SC1); }
DI void glds(rsrc_t r, char LAS* dst, u32 v, u32 s){ __builtin_amdgcn_raw_ptr_buffer_load_lds(r, (LAS void*)dst, 16, v, s, 0, 0); }
DI f32x16 mfma(u32x4 a, u32x4 b, f32x16 c){ return __builtin_amdgcn_mfma_f32_32x32x16_bf16(__builtin_bit_cast(bf16x8,a), __builtin_bit_cast(bf16x8,b), c, 0,0,0); }
DI u32x4 trfrag(const char LAS* p0, const char LAS* p1){
  s16x4 a=__builtin_amdgcn_ds_read_tr16_b64_v4i16((LAS s16x4*)p0);
  s16x4 b=__builtin_amdgcn_ds_read_tr16_b64_v4i16((LAS s16x4*)p1);
  return __builtin_bit_cast(u32x4, __builtin_shufflevector(a,b,0,1,2,3,4,5,6,7));
}
DI u32 pk2(f32x2 v){ return __builtin_bit_cast(u32, __builtin_convertvector(v, bf16x2)); }
DI u32 pk(float a, float b){ return pk2(f32x2{a,b}); }
DI float lo(u32 v){ return __uint_as_float(v<<16); }
DI float hi(u32 v){ return __uint_as_float(v&0xffff0000u); }
DI f32x2 un(u32 v){ return f32x2{lo(v),hi(v)}; }
DI f32x2 pr(const f32x16& a, int i){ return f32x2{a[2*i],a[2*i+1]}; }
DI u32x4 pack8(const f32x16& a, int s){ u32x4 r={pk2(pr(a,4*s)),pk2(pr(a,4*s+1)),pk2(pr(a,4*s+2)),pk2(pr(a,4*s+3))}; return r; }
DI float relu(float t){ return __int_as_float(max(__float_as_int(t),0)); }
DI u32 phi2(f32x2 t){ f32x2 sc=t*1.44269504089f;
  f32x2 e={__builtin_amdgcn_fmed3f(__builtin_amdgcn_exp2f(sc.x),0.f,1.f), __builtin_amdgcn_fmed3f(__builtin_amdgcn_exp2f(sc.y),0.f,1.f)};
  f32x2 m={relu(t.x),relu(t.y)}; return pk2(m+e+1e-6f); }
DI u32x4 phi8(const f32x16& a, int s){ u32x4 r={phi2(pr(a,4*s)),phi2(pr(a,4*s+1)),phi2(pr(a,4*s+2)),phi2(pr(a,4*s+3))}; return r; }
DI u32 sq2(f32x2 t){ f32x2 m={relu(t.x),relu(t.y)}; return pk2(m*m); }
DI float dot2(u32 a, u32 b, float c){ return __builtin_amdgcn_fdot2_f32_bf16(__builtin_bit_cast(bf16x2,a),__builtin_bit_cast(bf16x2,b),c,false); }
DI float quad_sum(float q){
  q+=__uint_as_float(__builtin_amdgcn_update_dpp(0,__float_as_uint(q),0xB1,0xf,0xf,true));
  q+=__uint_as_float(__builtin_amdgcn_update_dpp(0,__float_as_uint(q),0x4E,0xf,0xf,true)); return q; }
DI float xhalf_sum(float v){ auto r=__builtin_amdgcn_permlane32_swap(__float_as_uint(v),__float_as_uint(v),false,false); return __uint_as_float(r[0])+__uint_as_float(r[1]); }
DI float bcast_hi(float v){ auto r=__builtin_amdgcn_permlane32_swap(__float_as_uint(v),__float_as_uint(v),false,false); return __uint_as_float(r[1]); }
#define BAR()  asm volatile("s_barrier" ::: "memory")
#define BARL() asm volatile("s_waitcnt lgkmcnt(0)\n\ts_barrier" ::: "memory")
#define BARV() asm volatile("s_waitcnt vmcnt(0)\n\ts_barrier" ::: "memory")
#define WAITV(n) asm volatile("s_waitcnt vmcnt(" #n ")" ::: "memory")
#define SB() __builtin_amdgcn_sched_barrier(0)

constexpr int LDS_BYTES = 163840;
constexpr int A_WKV=0, A_X=100352, P0=0, P1=57344, SLOT=16384;

#define STAMP(i)
#define STAMP_DECL
#define STAMP_FLUSH

DI u32 img_src(int l, int rowstride){ int row=l>>3, pos=l&7; return row*rowstride + ((pos^(((row>>1)&1)<<2))*16); }
DI int img_rd(int l, int nt){ int hh=l>>5, cb=(l>>4)&1, q=(l&15)>>2, p=l&3; return (4*hh+q)*128 + ((nt^(q>>1))*64) + cb*32 + p*8; }

DI void send(char* p, u64 tag){ asm volatile("global_atomic_swap_x2 %0, %1, off sc1" :: "v"(p), "v"(tag) : "memory"); }
DI void poll(char* p, bool act, u64 tag){
  if(act){
    u64 f0,f1,z=0;
    #define ISS(f) asm volatile("global_atomic_or_x2 %0, %1, %2, off sc0 sc1\n\ts_sleep 3" : "=&v"(f) : "v"(p), "v"(z) : "memory")
    #define WT(f)  asm volatile("s_waitcnt vmcnt(1)" : "+v"(f) :: "memory")
    ISS(f0);
    for(int spins=0;spins<20000;spins++){
      ISS(f1); WT(f0); if(__all(f0==tag)) break;
      ISS(f0); WT(f1); if(__all(f1==tag)) break;
    }
    asm volatile("s_barrier\n\ts_waitcnt vmcnt(0)" : "+v"(f0),"+v"(f1) :: "memory");
  }
}

__global__ void __launch_bounds__(448,2) fused(const u16* x, const u16* wqkv, const u16* wo, const u16* w1, const u16* b1, const u16* w2, const u16* b2, char* ws, u16* out){
  extern __shared__ __attribute__((aligned(16))) char smem_[];
  char LAS* smem=(char LAS*)smem_;
  const int b=blockIdx.x, t=(b&7)*4+((b>>3)&3), j=b>>5;
  const int w=__builtin_amdgcn_readfirstlane(threadIdx.x>>6), l=threadIdx.x&63;
  const int rot=t%7, c=t>>1, fc=(t&1)*7+j;
  if(w==6){
    const u64 tag=__hip_atomic_load((u64*)(ws+WS_EPOCH), __ATOMIC_RELAXED, __HIP_MEMORY_SCOPE_AGENT)+1;
    BAR(); BAR(); BAR(); BAR();
    BAR();
    if(l==0) send(ws+WS_IB1+j*256+t*8, tag);
    BAR();
    BAR();
    if(l<2) send(ws+WS_IB3+(t*2+l)*256+j*8, tag);
    if(j==7){
      BAR();
      BAR();
      poll(ws+WS_IB1+(t>>2)*256+l*8, l<32, tag);
      BAR();
      BAR();
      if(l<56) send(ws+WS_IB2+l*256+t*8, tag);
      return;
    }
    poll(ws+WS_IB3+(t*2+(j>>2))*256+l*8, l<8, tag);
    poll(ws+WS_IB2+(j*8+(t>>2))*256+l*8, l<32, tag);
    BAR();
    BAR();
    if(l<4) send(ws+WS_IB4+(c*4+l)*256+fc*8, tag);
    poll(ws+WS_IB4+(c*4+(fc>>2))*256+l*8, l<14, tag);
    if(b==0 && l==0) __hip_atomic_store((u64*)(ws+WS_EPOCH), tag, __ATOMIC_RELAXED, __HIP_MEMORY_SCOPE_AGENT);
    BAR();
    BAR();
    if(l<4) send(ws+WS_IB4+(64+c*4+l)*256+fc*8, tag);
    poll(ws+WS_IB4+(64+c*4+(fc>>2))*256+l*8, l<14, tag);
    BAR();
    if(l<4) send(ws+WS_IB4+(128+c*4+l)*256+fc*8, tag);
    poll(ws+WS_IB4+(128+c*4+(fc>>2))*256+l*8, l<14, tag);
    return;
  }
  if(w>=4){
    const int lw=w-4;
    { rsrc_t rw=mk(wqkv, D*3*D*2);
      u32 pk_[7], pq_[7];
      #pragma unroll
      for(int i=0;i<7;i++){ int cc=i*64+l, blk=cc/112, rem=cc%112, q=rem/28, r28=rem%28;
        pk_[i]=(8*blk+4*(r28/14)+q)*2688 + (1+(r28%14)/7)*896 + (r28%7)*16;
        pq_[i]=(16*blk+4*(r28/7)+q)*2688 + (r28%7)*16; }
      auto kvper=[&](int ch){ int gch=ch+rot; if(gch>=7) gch-=7; u32 so=(gch*64+lw*32)*2688+j*112;
        #pragma unroll
        for(int i=0;i<7;i++) glds(rw, smem+A_WKV+(2*ch+lw)*7168+i*1024, pk_[i], so); };
      auto qper=[&](int ch){ int gch=ch+rot; if(gch>=7) gch-=7; u32 so=gch*64*2688+j*112;
        #pragma unroll
        for(int i=0;i<7;i++) glds(rw, smem+A_X+ch*7168+i*1024, pq_[i], so); };
      #pragma unroll 1
      for(int ch=0;ch<5;ch++) kvper(ch);
      WAITV(14); BAR();
      #pragma unroll 1
      for(int ch=5;ch<7;ch++) kvper(ch);
      WAITV(14); BAR();
      qper(lw);
      WAITV(14); BAR();
      WAITV(7); BAR();
      #pragma unroll 1
      for(int ch=2+lw;ch<7;ch+=2) qper(ch);
      BAR();
      WAITV(0); BAR();
    }
    const u32 s896=img_src(l,896), s3584=img_src(l,3584);
    rsrc_t rw1=mk(w1, D*F*2);
    if(j==7){
      if(t<28){
        #pragma unroll 4
        for(int i=0;i<28;i++){ int blk=2*i+lw; glds(rw1, smem+P0+blk*1024, s3584, blk*8*3584+t*128); } }
      BAR();
      WAITV(0); BAR();
      BAR(); BAR(); BAR(); BAR();
      return;
    }
    rsrc_t rwo=mk(wo, D*D*2), rw2=mk(w2, D*F*2), rx=mk(x, S*D*2);
    auto w2stage=[&](int st, int slot){ const int hf=st>=7, i7=st-7*hf; const u32 so=((7*lw+i7)*128+hf*64)*896+j*128;
      #pragma unroll
      for(int i=0;i<8;i++) glds(rw2, smem+slot*SLOT+lw*8192+i*1024, s896, so+i*8*896); };
    #pragma unroll 4
    for(int i=0;i<28;i++){ int blk=2*i+lw; glds(rwo, smem+P0+blk*1024, s896, blk*8*896+j*128); }
    #pragma unroll 3
    for(int i=0;i<21;i++){ int blk=2*i+lw; glds(rw1, smem+P1+blk*1024, s3584, blk*8*3584+fc*256); }
    BAR();
    { const int row=l>>3; const u32 sx=row*896+(((l&7)^row)*16);
      #pragma unroll
      for(int i=0;i<4;i++){ int blk=2*i+lw; glds(rx, smem+9*SLOT+blk*1024, sx, (t*64+blk*8)*896+j*128); } }
    #pragma unroll
    for(int i=21;i<28;i++){ int blk=2*i+lw; glds(rw1, smem+P1+blk*1024, s3584, blk*8*3584+fc*256); }
    WAITV(7); BAR();
    w2stage(0,7); w2stage(1,8);
    BAR();
    BAR();
    #pragma unroll 4
    for(int i=0;i<28;i++){ int blk=2*i+lw; glds(rw1, smem+P0+blk*1024, s3584, blk*8*3584+fc*256+128); }
    BAR();
    w2stage(2,9);
    WAITV(36); BAR();
    WAITV(0); BAR();
    w2stage(3,4); w2stage(4,5);
    BAR();
    w2stage(5,6);
    WAITV(0); BAR();
    BAR();
    w2stage(6,0); w2stage(7,1);
    BAR();
    w2stage(8,2); w2stage(9,3);
    WAITV(16); BAR();
    w2stage(10,7); w2stage(11,8);
    WAITV(16); BAR();
    w2stage(12,9); w2stage(13,4);
    WAITV(16); BAR();
    WAITV(0); BAR();
    return;
  }
  STAMP_DECL
  STAMP(0);
  rsrc_t rs=mk(ws, WS_END);
  const int th=w>>1, hd=w&1, kh=w&1, hh=l>>5, r=l&31, tt=2*t+th;
  {
    rsrc_t rx=mk(x, S*D*2);
    { const int row=16*w+(l>>2), q=l&3;
      u32x4 raw[14];
      #pragma unroll
      for(int i=0;i<14;i++){ int ch=(i>>1)+rot; if(ch>=7) ch-=7; raw[i]=ld16(rx, row*896+q*16+(i&1)*64, t*64*896+ch*128); }
      SB();
      float s1=0,s2=0,s1b=0,s2b=0;
      #pragma unroll
      for(int i=0;i<14;i++){
        s1=dot2(raw[i][0],0x3f803f80u,s1); s2=dot2(raw[i][0],raw[i][0],s2); s1b=dot2(raw[i][1],0x3f803f80u,s1b); s2b=dot2(raw[i][1],raw[i][1],s2b);
        s1=dot2(raw[i][2],0x3f803f80u,s1); s2=dot2(raw[i][2],raw[i][2],s2); s1b=dot2(raw[i][3],0x3f803f80u,s1b); s2b=dot2(raw[i][3],raw[i][3],s2b);
        SB(); }
      STAMP(1);
      const float m=quad_sum(s1+s1b)*(1.f/448);
      const float var=fmaxf(quad_sum(s2+s2b)*(1.f/448)-m*m,0.f);
      const float rs_=__builtin_amdgcn_rsqf(var+1e-5f), sh=-m*rs_;
      char LAS* xw=smem+A_X+(th*56+q)*512+(((row&31)^(2*q))*16);
      #pragma unroll
      for(int i=0;i<14;i++){ u32x4 o;
        #pragma unroll
        for(int e=0;e<4;e++) o[e]=pk2(__builtin_elementwise_fma(un(raw[i][e]), f32x2{rs_,rs_}, f32x2{sh,sh}));
        *(LAS u32x4*)(xw+i*2048)=o; SB(); }
    }
    STAMP(2);
    BARL();
    STAMP(3);
    u32x4 xf[28];
    const char LAS* x0=smem+A_X+(th*56+hh)*512+((r^(2*hh))*16);
    const char LAS* x1=smem+A_X+(th*56+hh)*512+((r^(4+2*hh))*16);
    #define XRD(s) xf[s]=*(const LAS u32x4*)((((s)&1)?x1:x0)+(s)*1024)
    const int q=(l&15)>>2, p=l&3, cb=(l>>4)&1;
    const char LAS* wb=smem+A_WKV+hh*1792+q*448+hd*56+cb*32+p*8;
    const char LAS* wb2=wb+14*3584;
    f32x16 aK={}, aV={};
    u32x4 fk[28], fv[28];
    #define WADDR(s) (((s)<14?wb:wb2)+((s)<14?(s):(s)-14)*3584)
    #define RDK(s) fk[s]=trfrag(WADDR(s),WADDR(s)+224)
    #define RDV(s) fv[s]=trfrag(WADDR(s)+112,WADDR(s)+336)
    #define KVSEG(s0,s1,XR) { _Pragma("unroll") for(int s=s0;s<s0+2;s++){ if(XR) XRD(s); RDK(s); RDV(s); } SB(); \
      _Pragma("unroll") for(int s=s0;s<s1;s++){ \
        if(XR){ if(s+2<12) XRD(s+2); } if(s+2<s1) RDK(s+2); aK=mfma(xf[s],fk[s],aK); SB(); \
        if(XR){ XRD(12+s); if(s>=8) XRD(16+s); } if(s+2<s1) RDV(s+2); aV=mfma(xf[s],fv[s],aV); SB(); } }
    KVSEG(0,12,1)
    BARL();
    KVSEG(12,20,0)
    BAR();
    KVSEG(20,24,0)
    BAR();
    STAMP(4);
    KVSEG(24,28,0)
    const int head=2*j+hd;
    { u32x4 k0=phi8(aK,0), k1=phi8(aK,1), v0=pack8(aV,0), v1=pack8(aV,1);
      const u32 vp = r==28? 0x3f803f80u : 0u;
      #pragma unroll
      for(int e=0;e<4;e++){ if(r>=28){ k0[e]=0; k1[e]=0; v0[e]=vp; v1[e]=vp; } }
      f32x16 kv={}; kv=mfma(k0,v0,kv); kv=mfma(k1,v1,kv);
      const u32 o=WS_KVP+(head*128+tt)*1024+l*16;
      st16c(pack8(kv,0), rs, o); st16c(pack8(kv,1), rs, o+65536);
    }
    STAMP(5);
    BARV();
    STAMP(6);
    BAR();
    { const char LAS* qb=smem+A_X+q*448+2*hh*112+hd*56+cb*32+p*8;
      f32x16 aQ={}; u32x4 fq[28];
      #pragma unroll
      for(int s=0;s<4;s++) fq[s]=trfrag(qb+s*1792,qb+s*1792+112);
      SB();
      #pragma unroll
      for(int s=0;s<28;s++){ if(s+4<28) fq[s+4]=trfrag(qb+(s+4)*1792,qb+(s+4)*1792+112); aQ=mfma(fq[s],xf[s],aQ); SB(); }
      u32x4 q0=phi8(aQ,0), q1=phi8(aQ,1);
      if(hh){ q1[2]=0; q1[3]=0; }
      const u32 o=WS_PQ+(tt*16+head)*2048+l*16;
      st16c(q0, rs, o); st16c(q1, rs, o+1024);
    }
    STAMP(7);
    BARV();
  }
  const char LAS* i0=smem+img_rd(l,0); const char LAS* i1=smem+img_rd(l,1);
  if(j==7){
    LAS float* red=(LAS float*)(smem+P1);
    BAR();
    u32 bb=0;
    if(t<28){
      bb=b1[t*64+hd*32+r];
      const char LAS* a=(hd?i1:i0)+P0+th*14*2048;
      const u32x4 ones={0x3f803f80u,0x3f803f80u,0x3f803f80u,0x3f803f80u};
      f32x16 cs={};
      #pragma unroll
      for(int s=0;s<14;s++) cs=mfma(ones,trfrag(a+s*2048,a+s*2048+1024),cs);
      red[w*64+l]=cs[0];
    }
    BARL();
    if(t<28 && w<2){
      const float s=red[w*64+r]+red[(w+2)*64+r];
      u32 ch=pk(s,s); float rem=s-lo(ch); u32 cl=pk(rem,0.f);
      u32x4 o={ch,cl,bb|(bb<<16),0u}; if(hh) o=u32x4{0,0,0,0};
      st16c(o, rs, WS_AUG+(t*2+w)*1024+l*16);
    }
    STAMP(8);
    BAR();
    STAMP(9);
    u32x4 v[16];
    #pragma unroll
    for(int i=0;i<16;i++) v[i]=ld16c(rs, l*16+(i&3)*1024, WS_KVP+t*65536+(w*16+(i&~3))*1024);
    SB();
    f32x2 a[4]={};
    #pragma unroll
    for(int i=0;i<16;i++){
      #pragma unroll
      for(int e=0;e<4;e++) a[e]+=un(v[i][e]);
      SB(); }
    LAS f32x4* r4=(LAS f32x4*)(smem+P1+4096);
    if(w){ r4[(w*2)*64+l]=f32x4{a[0].x,a[0].y,a[1].x,a[1].y}; r4[(w*2+1)*64+l]=f32x4{a[2].x,a[2].y,a[3].x,a[3].y}; }
    STAMP(10);
    BARL();
    if(w==0){
      #pragma unroll
      for(int k=1;k<4;k++){ f32x4 p0=r4[(k*2)*64+l], p1=r4[(k*2+1)*64+l];
        a[0]+=f32x2{p0.x,p0.y}; a[1]+=f32x2{p0.z,p0.w}; a[2]+=f32x2{p1.x,p1.y}; a[3]+=f32x2{p1.z,p1.w}; }
      st16c(u32x4{pk2(a[0]),pk2(a[1]),pk2(a[2]),pk2(a[3])}, rs, WS_KVT+t*1024+l*16);
    }
    BARV();
    STAMP(11);
    STAMP_FLUSH;
    return;
  }
  f32x16 hres;
  {
    BAR();
    STAMP(8);
    u32x4 pq[16], kv[16]; u32x2 xr[4];
    #pragma unroll
    for(int i=0;i<16;i++) pq[i]=ld16c(rs, l*16+(i&3)*1024, WS_PQ+tt*32768+kh*16384+(i&~3)*1024);
    #pragma unroll
    for(int gq=0;gq<4;gq++) xr[gq]=*(const LAS u32x2*)(smem+9*SLOT+(th*32+r)*128+(((kh*4+gq)^(r&7))*16)+hh*8);
    BARL();
    STAMP(9);
    #pragma unroll
    for(int i=0;i<16;i++) kv[i]=ld16c(rs, l*16+(i&3)*1024, WS_KVT+kh*16384+(i&~3)*1024);
    SB();
    const char LAS* w0=i0+P0+kh*8*28*128; const char LAS* w1_=i1+P0+kh*8*28*128;
    f32x16 acc0={}, acc1={};
    f32x16 num[8]; u32x4 o0[8], o1[8];
    const f32x16 zero={};
    #define NUM(h) { num[h]=mfma(kv[2*(h)],pq[2*(h)],zero); num[h]=mfma(kv[2*(h)+1],pq[2*(h)+1],num[h]); }
    #define FIN(h) { const float rc=__builtin_amdgcn_rcpf(bcast_hi(num[h][12])+1e-6f); f32x16 n_=num[h]*rc; o0[h]=pack8(n_,0); o1[h]=pack8(n_,1); if(hh){ o1[h][2]=0; o1[h][3]=0; } }
    NUM(0) NUM(1) SB(); FIN(0) SB();
    #pragma unroll
    for(int h=0;h<8;h++){
      const int ro=h*28*128;
      u32x4 fa=trfrag(w0+ro,w0+ro+1024), fb=trfrag(w1_+ro,w1_+ro+1024), fc_=trfrag(w0+ro+2048,w0+ro+3072), fd=trfrag(w1_+ro+2048,w1_+ro+3072);
      if(h+2<8) NUM(h+2)
      SB();
      acc0=mfma(fa,o0[h],acc0); acc1=mfma(fb,o0[h],acc1); acc0=mfma(fc_,o1[h],acc0); acc1=mfma(fd,o1[h],acc1);
      if(h+1<8) FIN(h+1)
      __builtin_amdgcn_sched_group_barrier(0x8,1,0); __builtin_amdgcn_sched_group_barrier(0x402,6,0);
      __builtin_amdgcn_sched_group_barrier(0x8,1,0); __builtin_amdgcn_sched_group_barrier(0x402,6,0);
      __builtin_amdgcn_sched_group_barrier(0x8,1,0); __builtin_amdgcn_sched_group_barrier(0x402,6,0);
      __builtin_amdgcn_sched_group_barrier(0x8,1,0); __builtin_amdgcn_sched_group_barrier(0x402,6,0);
      SB();
    }
    f32x16 keep, sendv;
    if(kh){ keep=acc1; sendv=acc0; } else { keep=acc0; sendv=acc1; }
    #pragma unroll
    for(int i=0;i<4;i++) *(LAS f32x4*)(smem+9*SLOT+w*4096+i*1024+l*16)=f32x4{sendv[4*i],sendv[4*i+1],sendv[4*i+2],sendv[4*i+3]};
    STAMP(10);
    BARL();
    f32x2 s1={0,0}, s2={0,0};
    #pragma unroll
    for(int i=0;i<4;i++){ f32x4 o=*(const LAS f32x4*)(smem+9*SLOT+(w^1)*4096+i*1024+l*16);
      f32x2 va=pr(keep,2*i)+un(xr[i][0])+f32x2{o.x,o.y}, vb=pr(keep,2*i+1)+un(xr[i][1])+f32x2{o.z,o.w};
      hres[4*i]=va.x; hres[4*i+1]=va.y; hres[4*i+2]=vb.x; hres[4*i+3]=vb.y;
      s1+=va; s1+=vb; s2=__builtin_elementwise_fma(va,va,s2); s2=__builtin_elementwise_fma(vb,vb,s2); }
    const u32 o=WS_H+(tt*28+4*j+2*kh)*1024+l*16;
    st16c(pack8(hres,0), rs, o); st16c(pack8(hres,1), rs, o+1024);
    const float t1=xhalf_sum(s1.x+s1.y), t2=xhalf_sum(s2.x+s2.y);
    if(hh==0) st8c(u32x2{__float_as_uint(t1),__float_as_uint(t2)}, rs, WS_ST+(tt*14+2*j+kh)*256+r*8);
    STAMP(11);
    BARV();
  }
  auto stats=[&](int tile, float& mu, float& var){
    u32x2 st[7];
    #pragma unroll
    for(int i=0;i<7;i++) st[i]=ld8c(rs, r*8+hh*7*256+i*256, WS_ST+tile*14*256);
    f32x2 s={0,0};
    #pragma unroll
    for(int i=0;i<7;i++) s+=f32x2{__uint_as_float(st[i][0]),__uint_as_float(st[i][1])};
    mu=xhalf_sum(s.x)*(1.f/448); var=fmaxf(xhalf_sum(s.y)*(1.f/448)-mu*mu,0.f)+1e-5f; };
  float rv;
  {
    const int tf=4*c+w;
    u32x4 aug[4];
    #pragma unroll
    for(int i=0;i<4;i++) aug[i]=ld16c(rs, l*16+i*1024, WS_AUG+fc*4096);
    BAR();
    STAMP(12);
    u32x4 hf_[28];
    #pragma unroll
    for(int s=0;s<8;s++) hf_[s]=ld16c(rs, l*16+(s&3)*1024, WS_H+(tf*28+(s&~3))*1024);
    u32x4 fa[56], fb[56];
    #define FADDR(base,s) ((base)+((s)<28?P1:P0)+((s)%28)*2048)
    #define RDA(s) fa[s]=trfrag(FADDR(i0,s),FADDR(i0,s)+1024)
    #define RDB(s) fb[s]=trfrag(FADDR(i1,s),FADDR(i1,s)+1024)
    RDA(0); RDB(0); RDA(1); RDB(1); RDA(2); RDB(2);
    SB();
    f32x16 a0={}, a1={};
    float mu,var; u32x2 st[7];
    #pragma unroll
    for(int s=0;s<28;s++){
      if(s+8<28) hf_[s+8]=ld16c(rs, l*16+((s+8)&3)*1024, WS_H+(tf*28+((s+8)&~3))*1024);
      else if(s-20<7) st[s-20]=ld8c(rs, r*8+hh*7*256+(s-20)*256, WS_ST+tf*14*256);
      if(s+3<28) RDA(s+3);
      a0=mfma(fa[s],hf_[s],a0); SB();
      if(s+3<28) RDB(s+3);
      a1=mfma(fb[s],hf_[s],a1); SB();
    }
    STAMP(13);
    BAR();
    RDA(28); RDB(28); RDA(29); RDB(29); RDA(30); RDB(30);
    u32x4 bs;
    { f32x2 s={0,0};
      #pragma unroll
      for(int i=0;i<7;i++) s+=f32x2{__uint_as_float(st[i][0]),__uint_as_float(st[i][1])};
      mu=xhalf_sum(s.x)*(1.f/448); var=fmaxf(xhalf_sum(s.y)*(1.f/448)-mu*mu,0.f)+1e-5f;
      const float sg=__builtin_amdgcn_sqrtf(var); u32 mh=pk(-mu,-mu); float mr=-mu-lo(mh); u32 sh=pk(sg,sg); float sr=sg-lo(sh);
      bs=u32x4{pk(lo(mh),mr), mh&0xffffu, pk(lo(sh),sr), 0u}; if(hh) bs=u32x4{0,0,0,0}; }
    a0=mfma(aug[0],bs,a0); a1=mfma(aug[1],bs,a1);
    const f32x16 zero={};
    f32x16 b0=mfma(aug[2],bs,zero), b1_=mfma(aug[3],bs,zero);
    SB();
    const u32 ho=WS_H1+(tf*112+(fc>=7?56:0)+(fc>=7?fc-7:fc)*4)*1024+l*16;
    u32x4 pa;
    #pragma unroll
    for(int s=0;s<28;s++){
      if(s+3<28) RDA(28+s+3);
      b0=mfma(fa[28+s],hf_[s],b0);
      if(s<8){ const int e=2*s; pa[e&3]=sq2(pr(s<4?a0:a1,e&7)); }
      SB();
      if(s+3<28) RDB(28+s+3);
      b1_=mfma(fb[28+s],hf_[s],b1_);
      if(s<8){ const int e=2*s+1; pa[e&3]=sq2(pr(s<4?a0:a1,e&7)); if(s&1) st16c(pa, rs, ho+(s>>1)*1024); }
      SB();
      if(s==21){ BARV(); }
    }
    STAMP(14);
    #pragma unroll
    for(int k=0;k<4;k++){ const f32x16& a=k<2?b0:b1_; const int e=(k&1)*4;
      st16c(u32x4{sq2(pr(a,e)),sq2(pr(a,e+1)),sq2(pr(a,e+2)),sq2(pr(a,e+3))}, rs, ho+28672+k*1024); }
    STAMP(15);
    BAR();
  }
  {
    STAMP(16);
    const u32 hb=WS_H1+(tt*112+kh*56)*1024;
    u32x4 a[56], fa[56], fb[56];
    #define LDH(s) a[s]=ld16c(rs, l*16+((s)&3)*1024, hb+((s)&~3)*1024)
    #pragma unroll
    for(int s=0;s<8;s++) LDH(s);
    WAITV(8); BAR();
    STAMP(17);
    #pragma unroll
    for(int s=8;s<12;s++) LDH(s);
    const char LAS* g0=i0+kh*8192; const char LAS* g1=i1+kh*8192;
    const char LAS* g0h=g0+4*SLOT; const char LAS* g1h=g1+4*SLOT;
    const char LAS* g0x=g0+8*SLOT; const char LAS* g1x=g1+8*SLOT;
    constexpr int slot[14]={7,8,9,4,5,6,0,1,2,3,7,8,9,4};
    #define GADDR(bl,bh,bx,s) ((slot[(s)>>2]<4?(bl):(slot[(s)>>2]<8?(bh):(bx)))+(slot[(s)>>2]&3)*SLOT+((s)&3)*2048)
    #define GRA(s) fa[s]=trfrag(GADDR(g0,g0h,g0x,s),GADDR(g0,g0h,g0x,s)+1024)
    #define GRB(s) fb[s]=trfrag(GADDR(g1,g1h,g1x,s),GADDR(g1,g1h,g1x,s)+1024)
    GRA(0); GRB(0); GRA(1); GRB(1); GRA(2); GRB(2);
    SB();
    f32x16 acc0={}, acc1={};
    u32x2 st[7], bv[4]; rsrc_t rb=mk(b2, D*2);
    #pragma unroll
    for(int s=0;s<56;s++){
      if(s==16){ BAR(); STAMP(18); }
      if(s==21||s==29||s==37||s==45){ BAR(); }
      if(s+12<56) LDH(s+12);
      else if(s-44<7) st[s-44]=ld8c(rs, r*8+hh*7*256+(s-44)*256, WS_ST+tt*14*256);
      else if(s-51<4) bv[s-51]=ld8(rb, hh*8+(s-51)*16, j*128+kh*64);
      if(s+3<56) GRA(s+3);
      acc0=mfma(fa[s],a[s],acc0); SB();
      if(s+3<56) GRB(s+3);
      acc1=mfma(fb[s],a[s],acc1); SB();
    }
    STAMP(19);
    f32x16 keep, sendv;
    if(kh){ keep=acc1; sendv=acc0; } else { keep=acc0; sendv=acc1; }
    #pragma unroll
    for(int i=0;i<4;i++) *(LAS f32x4*)(smem+5*SLOT+w*4096+i*1024+l*16)=f32x4{sendv[4*i],sendv[4*i+1],sendv[4*i+2],sendv[4*i+3]};
    { f32x2 s={0,0};
      #pragma unroll
      for(int i=0;i<7;i++) s+=f32x2{__uint_as_float(st[i][0]),__uint_as_float(st[i][1])};
      const float mu=xhalf_sum(s.x)*(1.f/448); rv=__builtin_amdgcn_rcpf(fmaxf(xhalf_sum(s.y)*(1.f/448)-mu*mu,0.f)+1e-5f); }
    BARL();
    u32 o[8];
    #pragma unroll
    for(int i=0;i<4;i++){ f32x4 p=*(const LAS f32x4*)(smem+5*SLOT+(w^1)*4096+i*1024+l*16);
      f32x2 va=(pr(keep,2*i)+f32x2{p.x,p.y})*rv+(un(bv[i][0])+pr(hres,2*i)), vb=(pr(keep,2*i+1)+f32x2{p.z,p.w})*rv+(un(bv[i][1])+pr(hres,2*i+1));
      o[2*i]=pk2(va); o[2*i+1]=pk2(vb); }
    rsrc_t ro_=mk(out, S*D*2);
    #pragma unroll
    for(int k=0;k<2;k++){
      auto r0=__builtin_amdgcn_permlane32_swap(o[4*k],o[4*k+2],false,false);
      auto r1=__builtin_amdgcn_permlane32_swap(o[4*k+1],o[4*k+3],false,false);
      st16(u32x4{r0[0],r1[0],r0[1],r1[1]}, ro_, (tt*32+r)*896+j*128+kh*64+hh*16+k*32);
    }
    STAMP(20);
    STAMP_FLUSH;
  }
}

extern "C" void kernel_launch(void* const* d_in, const int* in_sizes, int n_in, void* d_out, int out_size, void* d_ws, size_t ws_size, hipStream_t stream) {
  (void)hipFuncSetAttribute((const void*)fused, hipFuncAttributeMaxDynamicSharedMemorySize, LDS_BYTES);
  fused<<<256,448,LDS_BYTES,stream>>>((const u16*)d_in[0],(const u16*)d_in[1],(const u16*)d_in[2],(const u16*)d_in[4],(const u16*)d_in[5],(const u16*)d_in[6],(const u16*)d_in[7],(char*)d_ws,(u16*)d_out);
}
```

```cpp
#define PF_CODE 1
#define PF_W 1
#define PF_WS 1
#define CODE_BACK 0
#define CODE_N 4
#include <hip/hip_runtime.h>
#include <hip/hip_bf16.h>
typedef unsigned short u16;
typedef unsigned u32;
typedef unsigned long long u64;
typedef short s16x4 __attribute__((ext_vector_type(4)));
typedef __bf16 bf16x8 __attribute__((ext_vector_type(8)));
typedef __bf16 bf16x2 __attribute__((ext_vector_type(2)));
typedef float f32x16 __attribute__((ext_vector_type(16)));
typedef float f32x4 __attribute__((ext_vector_type(4)));
typedef float f32x2 __attribute__((ext_vector_type(2)));
typedef unsigned u32x4 __attribute__((ext_vector_type(4)));
typedef unsigned u32x2 __attribute__((ext_vector_type(2)));
#define LAS __attribute__((address_space(3)))
#define DI __device__ __forceinline__
typedef __amdgpu_buffer_rsrc_t rsrc_t;

constexpr int S=2048, D=448, F=1792;
constexpr u32 WS_EPOCH= 0;
constexpr u32 WS_IB1  = 1024;
constexpr u32 WS_IB2  = WS_IB1 + 8*256;
constexpr u32 WS_IB3  = WS_IB2 + 56*256;
constexpr u32 WS_IB4  = WS_IB3 + 64*256;
constexpr u32 WS_KVT  = 131072;
constexpr u32 WS_AUG  = WS_KVT + 32*1024;
constexpr u32 WS_ST   = WS_AUG + 64*1024;
constexpr u32 WS_PQ   = WS_ST + 64*14*256;
constexpr u32 WS_KVP  = WS_PQ + 64*32*1024;
constexpr u32 WS_H    = WS_KVP + 16*2*64*1024;
constexpr u32 WS_H1   = WS_H + 64*28*1024;
constexpr u32 WS_END  = WS_H1 + 64*112*1024;
static_assert(WS_IB4 + 3*64*256 <= WS_KVT, "inbox overflow");

constexpr int SC1=16;
DI rsrc_t mk(const void* p, u32 n){ return __builtin_amdgcn_make_buffer_rsrc((void*)p, 0, n, 0x00020000); }
DI u32x4 ld16(rsrc_t r, u32 v, u32 s){ return __builtin_amdgcn_raw_buffer_load_b128(r, v, s, 0); }
DI u32x2 ld8(rsrc_t r, u32 v, u32 s){ return __builtin_amdgcn_raw_buffer_load_b64(r, v, s, 0); }
DI u32x4 ld16c(rsrc_t r, u32 v, u32 s){ return __builtin_amdgcn_raw_buffer_load_b128(r, v, s, SC1); }
DI u32x2 ld8c(rsrc_t r, u32 v, u32 s){ return __builtin_amdgcn_raw_buffer_load_b64(r, v, s, SC1); }
DI void st16(u32x4 d, rsrc_t r, u32 v){ __builtin_amdgcn_raw_buffer_store_b128(d, r, v, 0, 0); }
DI void st16c(u32x4 d, rsrc_t r, u32 v){ __builtin_amdgcn_raw_buffer_store_b128(d, r, v, 0, SC1); }
DI void st8c(u32x2 d, rsrc_t r, u32 v){ __builtin_amdgcn_raw_buffer_store_b64(d, r, v, 0, SC1); }
DI void glds(rsrc_t r, char LAS* dst, u32 v, u32 s){ __builtin_amdgcn_raw_ptr_buffer_load_lds(r, (LAS void*)dst, 16, v, s, 0, 0); }
DI f32x16 mfma(u32x4 a, u32x4 b, f32x16 c){ return __builtin_amdgcn_mfma_f32_32x32x16_bf16(__builtin_bit_cast(bf16x8,a), __builtin_bit_cast(bf16x8,b), c, 0,0,0); }
DI u32x4 trfrag(const char LAS* p0, const char LAS* p1){
  s16x4 a=__builtin_amdgcn_ds_read_tr16_b64_v4i16((LAS s16x4*)p0);
  s16x4 b=__builtin_amdgcn_ds_read_tr16_b64_v4i16((LAS s16x4*)p1);
  return __builtin_bit_cast(u32x4, __builtin_shufflevector(a,b,0,1,2,3,4,5,6,7));
}
DI u32 pk2(f32x2 v){ return __builtin_bit_cast(u32, __builtin_convertvector(v, bf16x2)); }
DI u32 pk(float a, float b){ return pk2(f32x2{a,b}); }
DI float lo(u32 v){ return __uint_as_float(v<<16); }
DI float hi(u32 v){ return __uint_as_float(v&0xffff0000u); }
DI f32x2 un(u32 v){ return f32x2{lo(v),hi(v)}; }
DI f32x2 pr(const f32x16& a, int i){ return f32x2{a[2*i],a[2*i+1]}; }
DI u32x4 pack8(const f32x16& a, int s){ u32x4 r={pk2(pr(a,4*s)),pk2(pr(a,4*s+1)),pk2(pr(a,4*s+2)),pk2(pr(a,4*s+3))}; return r; }
DI float relu(float t){ return __int_as_float(max(__float_as_int(t),0)); }
DI u32 phi2(f32x2 t){ f32x2 sc=t*1.44269504089f;
  f32x2 e={__builtin_amdgcn_fmed3f(__builtin_amdgcn_exp2f(sc.x),0.f,1.f), __builtin_amdgcn_fmed3f(__builtin_amdgcn_exp2f(sc.y),0.f,1.f)};
  f32x2 m={relu(t.x),relu(t.y)}; return pk2(m+e+1e-6f); }
DI u32x4 phi8(const f32x16& a, int s){ u32x4 r={phi2(pr(a,4*s)),phi2(pr(a,4*s+1)),phi2(pr(a,4*s+2)),phi2(pr(a,4*s+3))}; return r; }
DI u32 sq2(f32x2 t){ f32x2 m={relu(t.x),relu(t.y)}; return pk2(m*m); }
DI float dot2(u32 a, u32 b, float c){ return __builtin_amdgcn_fdot2_f32_bf16(__builtin_bit_cast(bf16x2,a),__builtin_bit_cast(bf16x2,b),c,false); }
DI float quad_sum(float q){
  q+=__uint_as_float(__builtin_amdgcn_update_dpp(0,__float_as_uint(q),0xB1,0xf,0xf,true));
  q+=__uint_as_float(__builtin_amdgcn_update_dpp(0,__float_as_uint(q),0x4E,0xf,0xf,true)); return q; }
DI float xhalf_sum(float v){ auto r=__builtin_amdgcn_permlane32_swap(__float_as_uint(v),__float_as_uint(v),false,false); return __uint_as_float(r[0])+__uint_as_float(r[1]); }
DI float bcast_hi(float v){ auto r=__builtin_amdgcn_permlane32_swap(__float_as_uint(v),__float_as_uint(v),false,false); return __uint_as_float(r[1]); }
#define BAR()  asm volatile("s_barrier" ::: "memory")
#define BARL() asm volatile("s_waitcnt lgkmcnt(0)\n\ts_barrier" ::: "memory")
#define BARV() asm volatile("s_waitcnt vmcnt(0)\n\ts_barrier" ::: "memory")
#define WAITV(n) asm volatile("s_waitcnt vmcnt(" #n ")" ::: "memory")
#define SB() __builtin_amdgcn_sched_barrier(0)

constexpr int LDS_BYTES = 163840;
constexpr int A_WKV=0, A_X=100352, P0=0, P1=57344, SLOT=16384;

#define STAMP(i)
#define STAMP_DECL
#define STAMP_FLUSH

DI u32 img_src(int l, int rowstride){ int row=l>>3, pos=l&7; return row*rowstride + ((pos^(((row>>1)&1)<<2))*16); }
DI int img_rd(int l, int nt){ int hh=l>>5, cb=(l>>4)&1, q=(l&15)>>2, p=l&3; return (4*hh+q)*128 + ((nt^(q>>1))*64) + cb*32 + p*8; }

DI void send(char* p, u64 tag){ asm volatile("global_atomic_swap_x2 %0, %1, off sc1" :: "v"(p), "v"(tag) : "memory"); }
DI void poll(char* p, bool act, u64 tag){
  if(act){
    u64 f0,f1,z=0;
    #define ISS(f) asm volatile("global_atomic_or_x2 %0, %1, %2, off sc0 sc1\n\ts_sleep 3" : "=&v"(f) : "v"(p), "v"(z) : "memory")
    #define WT(f)  asm volatile("s_waitcnt vmcnt(1)" : "+v"(f) :: "memory")
    ISS(f0);
    for(int spins=0;spins<20000;spins++){
      ISS(f1); WT(f0); if(__all(f0==tag)) break;
      ISS(f0); WT(f1); if(__all(f1==tag)) break;
    }
    asm volatile("s_barrier\n\ts_waitcnt vmcnt(0)" : "+v"(f0),"+v"(f1) :: "memory");
  }
}

__global__ void __launch_bounds__(448,2) fused(const u16* x, const u16* wqkv, const u16* wo, const u16* w1, const u16* b1, const u16* w2, const u16* b2, char* ws, u16* out){
  extern __shared__ __attribute__((aligned(16))) char smem_[];
  char LAS* smem=(char LAS*)smem_;
  u64 pcv; asm volatile("s_getpc_b64 %0" : "=s"(pcv));
  const int b=blockIdx.x, t=(b&7)*4+((b>>3)&3), j=b>>5;
  const int w=__builtin_amdgcn_readfirstlane(threadIdx.x>>6), l=threadIdx.x&63;
  const int rot=t%7, c=t>>1, fc=(t&1)*7+j;
  if(w==6){
    const u64 tag=__hip_atomic_load((u64*)(ws+WS_EPOCH), __ATOMIC_RELAXED, __HIP_MEMORY_SCOPE_AGENT)+1;
    u32 junk=0;
    {
      const u32* p=(const u32*)((pcv&~127ull)-CODE_BACK);
      #pragma unroll
      for(int i=0;i<CODE_N;i++) junk+=p[(i*64+l)*32]; }
    {
      rsrc_t a=mk(wo,D*D*2), bq=mk(w1,D*F*2), cq=mk(w2,D*F*2);
      junk+=__builtin_amdgcn_raw_buffer_load_b32(a, (b*13+l)*128, 0, 0)*(l<13);
      junk+=__builtin_amdgcn_raw_buffer_load_b32(bq, (b*49+(l<49?l:0))*128, 0, 0);
      junk+=__builtin_amdgcn_raw_buffer_load_b32(cq, (b*49+(l<49?l:0))*128, 0, 0); }
    { rsrc_t a=mk(ws,WS_END), o=mk(out,S*D*2), b1r=mk(b1,F*2), b2r=mk(b2,D*2);
      junk+=__builtin_amdgcn_raw_buffer_load_b32(a, l*262144+126976, 0, 0);
      junk+=__builtin_amdgcn_raw_buffer_load_b32(o, l*65536, 0, 0);
      junk+=__builtin_amdgcn_raw_buffer_load_b32(b1r, l*128, 0, 0);
      junk+=__builtin_amdgcn_raw_buffer_load_b32(b2r, l*128, 0, 0); }
    asm volatile("" :: "v"(junk));
    BAR(); BAR(); BAR(); BAR();
    BAR();
    if(l==0) send(ws+WS_IB1+j*256+t*8, tag);
    BAR();
    BAR();
    if(l<2) send(ws+WS_IB3+(t*2+l)*256+j*8, tag);
    if(j==7){
      BAR();
      BAR();
      poll(ws+WS_IB1+(t>>2)*256+l*8, l<32, tag);
      BAR();
      BAR();
      if(l<56) send(ws+WS_IB2+l*256+t*8, tag);
      return;
    }
    poll(ws+WS_IB3+(t*2+(j>>2))*256+l*8, l<8, tag);
    poll(ws+WS_IB2+(j*8+(t>>2))*256+l*8, l<32, tag);
    BAR();
    BAR();
    if(l<4) send(ws+WS_IB4+(c*4+l)*256+fc*8, tag);
    poll(ws+WS_IB4+(c*4+(fc>>2))*256+l*8, l<14, tag);
    if(b==0 && l==0) __hip_atomic_store((u64*)(ws+WS_EPOCH), tag, __ATOMIC_RELAXED, __HIP_MEMORY_SCOPE_AGENT);
    BAR();
    BAR();
    if(l<4) send(ws+WS_IB4+(64+c*4+l)*256+fc*8, tag);
    poll(ws+WS_IB4+(64+c*4+(fc>>2))*256+l*8, l<14, tag);
    BAR();
    if(l<4) send(ws+WS_IB4+(128+c*4+l)*256+fc*8, tag);
    poll(ws+WS_IB4+(128+c*4+(fc>>2))*256+l*8, l<14, tag);
    return;
  }
  if(w>=4){
    const int lw=w-4;
    { rsrc_t rw=mk(wqkv, D*3*D*2);
      u32 pk_[7], pq_[7];
      #pragma unroll
      for(int i=0;i<7;i++){ int cc=i*64+l, blk=cc/112, rem=cc%112, q=rem/28, r28=rem%28;
        pk_[i]=(8*blk+4*(r28/14)+q)*2688 + (1+(r28%14)/7)*896 + (r28%7)*16;
        pq_[i]=(16*blk+4*(r28/7)+q)*2688 + (r28%7)*16; }
      auto kvper=[&](int ch){ int gch=ch+rot; if(gch>=7) gch-=7; u32 so=(gch*64+lw*32)*2688+j*112;
        #pragma unroll
        for(int i=0;i<7;i++) glds(rw, smem+A_WKV+(2*ch+lw)*7168+i*1024, pk_[i], so); };
      auto qper=[&](int ch){ int gch=ch+rot; if(gch>=7) gch-=7; u32 so=gch*64*2688+j*112;
        #pragma unroll
        for(int i=0;i<7;i++) glds(rw, smem+A_X+ch*7168+i*1024, pq_[i], so); };
      #pragma unroll 1
      for(int ch=0;ch<5;ch++) kvper(ch);
      WAITV(14); BAR();
      #pragma unroll 1
      for(int ch=5;ch<7;ch++) kvper(ch);
      WAITV(14); BAR();
      qper(lw);
      WAITV(14); BAR();
      WAITV(7); BAR();
      #pragma unroll 1
      for(int ch=2+lw;ch<7;ch+=2) qper(ch);
      BAR();
      WAITV(0); BAR();
    }
    const u32 s896=img_src(l,896), s3584=img_src(l,3584);
    rsrc_t rw1=mk(w1, D*F*2);
    if(j==7){
      if(t<28){
        #pragma unroll 4
        for(int i=0;i<28;i++){ int blk=2*i+lw; glds(rw1, smem+P0+blk*1024, s3584, blk*8*3584+t*128); } }
      BAR();
      WAITV(0); BAR();
      BAR(); BAR(); BAR(); BAR();
      return;
    }
    rsrc_t rwo=mk(wo, D*D*2), rw2=mk(w2, D*F*2), rx=mk(x, S*D*2);
    auto w2stage=[&](int st, int slot){ const int hf=st>=7, i7=st-7*hf; const u32 so=((7*lw+i7)*128+hf*64)*896+j*128;
      #pragma unroll
      for(int i=0;i<8;i++) glds(rw2, smem+slot*SLOT+lw*8192+i*1024, s896, so+i*8*896); };
    #pragma unroll 4
    for(int i=0;i<28;i++){ int blk=2*i+lw; glds(rwo, smem+P0+blk*1024, s896, blk*8*896+j*128); }
    #pragma unroll 3
    for(int i=0;i<21;i++){ int blk=2*i+lw; glds(rw1, smem+P1+blk*1024, s3584, blk*8*3584+fc*256); }
    BAR();
    { const int row=l>>3; const u32 sx=row*896+(((l&7)^row)*16);
      #pragma unroll
      for(int i=0;i<4;i++){ int blk=2*i+lw; glds(rx, smem+9*SLOT+blk*1024, sx, (t*64+blk*8)*896+j*128); } }
    #pragma unroll
    for(int i=21;i<28;i++){ int blk=2*i+lw; glds(rw1, smem+P1+blk*1024, s3584, blk*8*3584+fc*256); }
    WAITV(7); BAR();
    w2stage(0,7); w2stage(1,8);
    BAR();
    BAR();
    #pragma unroll 4
    for(int i=0;i<28;i++){ int blk=2*i+lw; glds(rw1, smem+P0+blk*1024, s3584, blk*8*3584+fc*256+128); }
    BAR();
    w2stage(2,9);
    WAITV(36); BAR();
    WAITV(0); BAR();
    w2stage(3,4); w2stage(4,5);
    BAR();
    w2stage(5,6);
    WAITV(0); BAR();
    BAR();
    w2stage(6,0); w2stage(7,1);
    BAR();
    w2stage(8,2); w2stage(9,3);
    WAITV(16); BAR();
    w2stage(10,7); w2stage(11,8);
    WAITV(16); BAR();
    w2stage(12,9); w2stage(13,4);
    WAITV(16); BAR();
    WAITV(0); BAR();
    return;
  }
  STAMP_DECL
  STAMP(0);
  rsrc_t rs=mk(ws, WS_END);
  const int th=w>>1, hd=w&1, kh=w&1, hh=l>>5, r=l&31, tt=2*t+th;
  {
    rsrc_t rx=mk(x, S*D*2);
    { const int row=16*w+(l>>2), q=l&3;
      u32x4 raw[14];
      #pragma unroll
      for(int i=0;i<14;i++){ int ch=(i>>1)+rot; if(ch>=7) ch-=7; raw[i]=ld16(rx, row*896+q*16+(i&1)*64, t*64*896+ch*128); }
      SB();
      float s1=0,s2=0,s1b=0,s2b=0;
      #pragma unroll
      for(int i=0;i<14;i++){
        s1=dot2(raw[i][0],0x3f803f80u,s1); s2=dot2(raw[i][0],raw[i][0],s2); s1b=dot2(raw[i][1],0x3f803f80u,s1b); s2b=dot2(raw[i][1],raw[i][1],s2b);
        s1=dot2(raw[i][2],0x3f803f80u,s1); s2=dot2(raw[i][2],raw[i][2],s2); s1b=dot2(raw[i][3],0x3f803f80u,s1b); s2b=dot2(raw[i][3],raw[i][3],s2b);
        SB(); }
      STAMP(1);
      const float m=quad_sum(s1+s1b)*(1.f/448);
      const float var=fmaxf(quad_sum(s2+s2b)*(1.f/448)-m*m,0.f);
      const float rs_=__builtin_amdgcn_rsqf(var+1e-5f), sh=-m*rs_;
      char LAS* xw=smem+A_X+(th*56+q)*512+(((row&31)^(2*q))*16);
      #pragma unroll
      for(int i=0;i<14;i++){ u32x4 o;
        #pragma unroll
        for(int e=0;e<4;e++) o[e]=pk2(__builtin_elementwise_fma(un(raw[i][e]), f32x2{rs_,rs_}, f32x2{sh,sh}));
        *(LAS u32x4*)(xw+i*2048)=o; SB(); }
    }
    STAMP(2);
    BARL();
    STAMP(3);
    u32x4 xf[28];
    const char LAS* x0=smem+A_X+(th*56+hh)*512+((r^(2*hh))*16);
    const char LAS* x1=smem+A_X+(th*56+hh)*512+((r^(4+2*hh))*16);
    #define XRD(s) xf[s]=*(const LAS u32x4*)((((s)&1)?x1:x0)+(s)*1024)
    const int q=(l&15)>>2, p=l&3, cb=(l>>4)&1;
    const char LAS* wb=smem+A_WKV+hh*1792+q*448+hd*56+cb*32+p*8;
    const char LAS* wb2=wb+14*3584;
    f32x16 aK={}, aV={};
    u32x4 fk[28], fv[28];
    #define WADDR(s) (((s)<14?wb:wb2)+((s)<14?(s):(s)-14)*3584)
    #define RDK(s) fk[s]=trfrag(WADDR(s),WADDR(s)+224)
    #define RDV(s) fv[s]=trfrag(WADDR(s)+112,WADDR(s)+336)
    #define KVSEG(s0,s1,XR) { _Pragma("unroll") for(int s=s0;s<s0+2;s++){ if(XR) XRD(s); RDK(s); RDV(s); } SB(); \
      _Pragma("unroll") for(int s=s0;s<s1;s++){ \
        if(XR){ if(s+2<12) XRD(s+2); } if(s+2<s1) RDK(s+2); aK=mfma(xf[s],fk[s],aK); SB(); \
        if(XR){ XRD(12+s); if(s>=8) XRD(16+s); } if(s+2<s1) RDV(s+2); aV=mfma(xf[s],fv[s],aV); SB(); } }
    KVSEG(0,12,1)
    BARL();
    KVSEG(12,20,0)
    BAR();
    KVSEG(20,24,0)
    BAR();
    STAMP(4);
    KVSEG(24,28,0)
    const int head=2*j+hd;
    { u32x4 k0=phi8(aK,0), k1=phi8(aK,1), v0=pack8(aV,0), v1=pack8(aV,1);
      const u32 vp = r==28? 0x3f803f80u : 0u;
      #pragma unroll
      for(int e=0;e<4;e++){ if(r>=28){ k0[e]=0; k1[e]=0; v0[e]=vp; v1[e]=vp; } }
      f32x16 kv={}; kv=mfma(k0,v0,kv); kv=mfma(k1,v1,kv);
      const u32 o=WS_KVP+(head*128+tt)*1024+l*16;
      st16c(pack8(kv,0), rs, o); st16c(pack8(kv,1), rs, o+65536);
    }
    STAMP(5);
    BARV();
    STAMP(6);
    BAR();
    { const char LAS* qb=smem+A_X+q*448+2*hh*112+hd*56+cb*32+p*8;
      f32x16 aQ={}; u32x4 fq[28];
      #pragma unroll
      for(int s=0;s<4;s++) fq[s]=trfrag(qb+s*1792,qb+s*1792+112);
      SB();
      #pragma unroll
      for(int s=0;s<28;s++){ if(s+4<28) fq[s+4]=trfrag(qb+(s+4)*1792,qb+(s+4)*1792+112); aQ=mfma(fq[s],xf[s],aQ); SB(); }
      u32x4 q0=phi8(aQ,0), q1=phi8(aQ,1);
      if(hh){ q1[2]=0; q1[3]=0; }
      const u32 o=WS_PQ+(tt*16+head)*2048+l*16;
      st16c(q0, rs, o); st16c(q1, rs, o+1024);
    }
    STAMP(7);
    BARV();
  }
  const char LAS* i0=smem+img_rd(l,0); const char LAS* i1=smem+img_rd(l,1);
  if(j==7){
    LAS float* red=(LAS float*)(smem+P1);
    BAR();
    u32 bb=0;
    if(t<28){
      bb=b1[t*64+hd*32+r];
      const char LAS* a=(hd?i1:i0)+P0+th*14*2048;
      const u32x4 ones={0x3f803f80u,0x3f803f80u,0x3f803f80u,0x3f803f80u};
      f32x16 cs={};
      #pragma unroll
      for(int s=0;s<14;s++) cs=mfma(ones,trfrag(a+s*2048,a+s*2048+1024),cs);
      red[w*64+l]=cs[0];
    }
    BARL();
    if(t<28 && w<2){
      const float s=red[w*64+r]+red[(w+2)*64+r];
      u32 ch=pk(s,s); float rem=s-lo(ch); u32 cl=pk(rem,0.f);
      u32x4 o={ch,cl,bb|(bb<<16),0u}; if(hh) o=u32x4{0,0,0,0};
      st16c(o, rs, WS_AUG+(t*2+w)*1024+l*16);
    }
    STAMP(8);
    BAR();
    STAMP(9);
    u32x4 v[16];
    #pragma unroll
    for(int i=0;i<16;i++) v[i]=ld16c(rs, l*16+(i&3)*1024, WS_KVP+t*65536+(w*16+(i&~3))*1024);
    SB();
    f32x2 a[4]={};
    #pragma unroll
    for(int i=0;i<16;i++){
      #pragma unroll
      for(int e=0;e<4;e++) a[e]+=un(v[i][e]);
      SB(); }
    LAS f32x4* r4=(LAS f32x4*)(smem+P1+4096);
    if(w){ r4[(w*2)*64+l]=f32x4{a[0].x,a[0].y,a[1].x,a[1].y}; r4[(w*2+1)*64+l]=f32x4{a[2].x,a[2].y,a[3].x,a[3].y}; }
    STAMP(10);
    BARL();
    if(w==0){
      #pragma unroll
      for(int k=1;k<4;k++){ f32x4 p0=r4[(k*2)*64+l], p1=r4[(k*2+1)*64+l];
        a[0]+=f32x2{p0.x,p0.y}; a[1]+=f32x2{p0.z,p0.w}; a[2]+=f32x2{p1.x,p1.y}; a[3]+=f32x2{p1.z,p1.w}; }
      st16c(u32x4{pk2(a[0]),pk2(a[1]),pk2(a[2]),pk2(a[3])}, rs, WS_KVT+t*1024+l*16);
    }
    BARV();
    STAMP(11);
    STAMP_FLUSH;
    return;
  }
  f32x16 hres;
  {
    BAR();
    STAMP(8);
    u32x4 pq[16], kv[16]; u32x2 xr[4];
    #pragma unroll
    for(int i=0;i<16;i++) pq[i]=ld16c(rs, l*16+(i&3)*1024, WS_PQ+tt*32768+kh*16384+(i&~3)*1024);
    #pragma unroll
    for(int gq=0;gq<4;gq++) xr[gq]=*(const LAS u32x2*)(smem+9*SLOT+(th*32+r)*128+(((kh*4+gq)^(r&7))*16)+hh*8);
    BARL();
    STAMP(9);
    #pragma unroll
    for(int i=0;i<16;i++) kv[i]=ld16c(rs, l*16+(i&3)*1024, WS_KVT+kh*16384+(i&~3)*1024);
    SB();
    const char LAS* w0=i0+P0+kh*8*28*128; const char LAS* w1_=i1+P0+kh*8*28*128;
    f32x16 acc0={}, acc1={};
    f32x16 num[8]; u32x4 o0[8], o1[8];
    const f32x16 zero={};
    #define NUM(h) { num[h]=mfma(kv[2*(h)],pq[2*(h)],zero); num[h]=mfma(kv[2*(h)+1],pq[2*(h)+1],num[h]); }
    #define FIN(h) { const float rc=__builtin_amdgcn_rcpf(bcast_hi(num[h][12])+1e-6f); f32x16 n_=num[h]*rc; o0[h]=pack8(n_,0); o1[h]=pack8(n_,1); if(hh){ o1[h][2]=0; o1[h][3]=0; } }
    NUM(0) NUM(1) SB(); FIN(0) SB();
    #pragma unroll
    for(int h=0;h<8;h++){
      const int ro=h*28*128;
      u32x4 fa=trfrag(w0+ro,w0+ro+1024), fb=trfrag(w1_+ro,w1_+ro+1024), fc_=trfrag(w0+ro+2048,w0+ro+3072), fd=trfrag(w1_+ro+2048,w1_+ro+3072);
      if(h+2<8) NUM(h+2)
      SB();
      acc0=mfma(fa,o0[h],acc0); acc1=mfma(fb,o0[h],acc1); acc0=mfma(fc_,o1[h],acc0); acc1=mfma(fd,o1[h],acc1);
      if(h+1<8) FIN(h+1)
      __builtin_amdgcn_sched_group_barrier(0x8,1,0); __builtin_amdgcn_sched_group_barrier(0x402,6,0);
      __builtin_amdgcn_sched_group_barrier(0x8,1,0); __builtin_amdgcn_sched_group_barrier(0x402,6,0);
      __builtin_amdgcn_sched_group_barrier(0x8,1,0); __builtin_amdgcn_sched_group_barrier(0x402,6,0);
      __builtin_amdgcn_sched_group_barrier(0x8,1,0); __builtin_amdgcn_sched_group_barrier(0x402,6,0);
      SB();
    }
    f32x16 keep, sendv;
    if(kh){ keep=acc1; sendv=acc0; } else { keep=acc0; sendv=acc1; }
    #pragma unroll
    for(int i=0;i<4;i++) *(LAS f32x4*)(smem+9*SLOT+w*4096+i*1024+l*16)=f32x4{sendv[4*i],sendv[4*i+1],sendv[4*i+2],sendv[4*i+3]};
    STAMP(10);
    BARL();
    f32x2 s1={0,0}, s2={0,0};
    #pragma unroll
    for(int i=0;i<4;i++){ f32x4 o=*(const LAS f32x4*)(smem+9*SLOT+(w^1)*4096+i*1024+l*16);
      f32x2 va=pr(keep,2*i)+un(xr[i][0])+f32x2{o.x,o.y}, vb=pr(keep,2*i+1)+un(xr[i][1])+f32x2{o.z,o.w};
      hres[4*i]=va.x; hres[4*i+1]=va.y; hres[4*i+2]=vb.x; hres[4*i+3]=vb.y;
      s1+=va; s1+=vb; s2=__builtin_elementwise_fma(va,va,s2); s2=__builtin_elementwise_fma(vb,vb,s2); }
    const u32 o=WS_H+(tt*28+4*j+2*kh)*1024+l*16;
    st16c(pack8(hres,0), rs, o); st16c(pack8(hres,1), rs, o+1024);
    const float t1=xhalf_sum(s1.x+s1.y), t2=xhalf_sum(s2.x+s2.y);
    if(hh==0) st8c(u32x2{__float_as_uint(t1),__float_as_uint(t2)}, rs, WS_ST+(tt*14+2*j+kh)*256+r*8);
    STAMP(11);
    BARV();
  }
  auto stats=[&](int tile, float& mu, float& var){
    u32x2 st[7];
    #pragma unroll
    for(int i=0;i<7;i++) st[i]=ld8c(rs, r*8+hh*7*256+i*256, WS_ST+tile*14*256);
    f32x2 s={0,0};
    #pragma unroll
    for(int i=0;i<7;i++) s+=f32x2{__uint_as_float(st[i][0]),__uint_as_float(st[i][1])};
    mu=xhalf_sum(s.x)*(1.f/448); var=fmaxf(xhalf_sum(s.y)*(1.f/448)-mu*mu,0.f)+1e-5f; };
  float rv;
  {
    const int tf=4*c+w;
    u32x4 aug[4];
    #pragma unroll
    for(int i=0;i<4;i++) aug[i]=ld16c(rs, l*16+i*1024, WS_AUG+fc*4096);
    BAR();
    STAMP(12);
    u32x4 hf_[28];
    #pragma unroll
    for(int s=0;s<8;s++) hf_[s]=ld16c(rs, l*16+(s&3)*1024, WS_H+(tf*28+(s&~3))*1024);
    u32x4 fa[56], fb[56];
    #define FADDR(base,s) ((base)+((s)<28?P1:P0)+((s)%28)*2048)
    #define RDA(s) fa[s]=trfrag(FADDR(i0,s),FADDR(i0,s)+1024)
    #define RDB(s) fb[s]=trfrag(FADDR(i1,s),FADDR(i1,s)+1024)
    RDA(0); RDB(0); RDA(1); RDB(1); RDA(2); RDB(2);
    SB();
    f32x16 a0={}, a1={};
    float mu,var; u32x2 st[7];
    #pragma unroll
    for(int s=0;s<28;s++){
      if(s+8<28) hf_[s+8]=ld16c(rs, l*16+((s+8)&3)*1024, WS_H+(tf*28+((s+8)&~3))*1024);
      else if(s-20<7) st[s-20]=ld8c(rs, r*8+hh*7*256+(s-20)*256, WS_ST+tf*14*256);
      if(s+3<28) RDA(s+3);
      a0=mfma(fa[s],hf_[s],a0); SB();
      if(s+3<28) RDB(s+3);
      a1=mfma(fb[s],hf_[s],a1); SB();
    }
    STAMP(13);
    BAR();
    RDA(28); RDB(28); RDA(29); RDB(29); RDA(30); RDB(30);
    u32x4 bs;
    { f32x2 s={0,0};
      #pragma unroll
      for(int i=0;i<7;i++) s+=f32x2{__uint_as_float(st[i][0]),__uint_as_float(st[i][1])};
      mu=xhalf_sum(s.x)*(1.f/448); var=fmaxf(xhalf_sum(s.y)*(1.f/448)-mu*mu,0.f)+1e-5f;
      const float sg=__builtin_amdgcn_sqrtf(var); u32 mh=pk(-mu,-mu); float mr=-mu-lo(mh); u32 sh=pk(sg,sg); float sr=sg-lo(sh);
      bs=u32x4{pk(lo(mh),mr), mh&0xffffu, pk(lo(sh),sr), 0u}; if(hh) bs=u32x4{0,0,0,0}; }
    a0=mfma(aug[0],bs,a0); a1=mfma(aug[1],bs,a1);
    const f32x16 zero={};
    f32x16 b0=mfma(aug[2],bs,zero), b1_=mfma(aug[3],bs,zero);
    SB();
    const u32 ho=WS_H1+(tf*112+(fc>=7?56:0)+(fc>=7?fc-7:fc)*4)*1024+l*16;
    u32x4 pa;
    #pragma unroll
    for(int s=0;s<28;s++){
      if(s+3<28) RDA(28+s+3);
      b0=mfma(fa[28+s],hf_[s],b0);
      if(s<8){ const int e=2*s; pa[e&3]=sq2(pr(s<4?a0:a1,e&7)); }
      SB();
      if(s+3<28) RDB(28+s+3);
      b1_=mfma(fb[28+s],hf_[s],b1_);
      if(s<8){ const int e=2*s+1; pa[e&3]=sq2(pr(s<4?a0:a1,e&7)); if(s&1) st16c(pa, rs, ho+(s>>1)*1024); }
      SB();
      if(s==21){ BARV(); }
    }
    STAMP(14);
    #pragma unroll
    for(int k=0;k<4;k++){ const f32x16& a=k<2?b0:b1_; const int e=(k&1)*4;
      st16c(u32x4{sq2(pr(a,e)),sq2(pr(a,e+1)),sq2(pr(a,e+2)),sq2(pr(a,e+3))}, rs, ho+28672+k*1024); }
    STAMP(15);
    BAR();
  }
  {
    STAMP(16);
    const u32 hb=WS_H1+(tt*112+kh*56)*1024;
    u32x4 a[56], fa[56], fb[56];
    #define LDH(s) a[s]=ld16c(rs, l*16+((s)&3)*1024, hb+((s)&~3)*1024)
    #pragma unroll
    for(int s=0;s<8;s++) LDH(s);
    WAITV(8); BAR();
    STAMP(17);
    #pragma unroll
    for(int s=8;s<12;s++) LDH(s);
    const char LAS* g0=i0+kh*8192; const char LAS* g1=i1+kh*8192;
    const char LAS* g0h=g0+4*SLOT; const char LAS* g1h=g1+4*SLOT;
    const char LAS* g0x=g0+8*SLOT; const char LAS* g1x=g1+8*SLOT;
    constexpr int slot[14]={7,8,9,4,5,6,0,1,2,3,7,8,9,4};
    #define GADDR(bl,bh,bx,s) ((slot[(s)>>2]<4?(bl):(slot[(s)>>2]<8?(bh):(bx)))+(slot[(s)>>2]&3)*SLOT+((s)&3)*2048)
    #define GRA(s) fa[s]=trfrag(GADDR(g0,g0h,g0x,s),GADDR(g0,g0h,g0x,s)+1024)
    #define GRB(s) fb[s]=trfrag(GADDR(g1,g1h,g1x,s),GADDR(g1,g1h,g1x,s)+1024)
    GRA(0); GRB(0); GRA(1); GRB(1); GRA(2); GRB(2);
    SB();
    f32x16 acc0={}, acc1={};
    u32x2 st[7], bv[4]; rsrc_t rb=mk(b2, D*2);
    #pragma unroll
    for(int s=0;s<56;s++){
      if(s==16){ BAR(); STAMP(18); }
      if(s==21||s==29||s==37||s==45){ BAR(); }
      if(s+12<56) LDH(s+12);
      else if(s-44<7) st[s-44]=ld8c(rs, r*8+hh*7*256+(s-44)*256, WS_ST+tt*14*256);
      else if(s-51<4) bv[s-51]=ld8(rb, hh*8+(s-51)*16, j*128+kh*64);
      if(s+3<56) GRA(s+3);
      acc0=mfma(fa[s],a[s],acc0); SB();
      if(s+3<56) GRB(s+3);
      acc1=mfma(fb[s],a[s],acc1); SB();
    }
    STAMP(19);
    f32x16 keep, sendv;
    if(kh){ keep=acc1; sendv=acc0; } else { keep=acc0; sendv=acc1; }
    #pragma unroll
    for(int i=0;i<4;i++) *(LAS f32x4*)(smem+5*SLOT+w*4096+i*1024+l*16)=f32x4{sendv[4*i],sendv[4*i+1],sendv[4*i+2],sendv[4*i+3]};
    { f32x2 s={0,0};
      #pragma unroll
      for(int i=0;i<7;i++) s+=f32x2{__uint_as_float(st[i][0]),__uint_as_float(st[i][1])};
      const float mu=xhalf_sum(s.x)*(1.f/448); rv=__builtin_amdgcn_rcpf(fmaxf(xhalf_sum(s.y)*(1.f/448)-mu*mu,0.f)+1e-5f); }
    BARL();
    u32 o[8];
    #pragma unroll
    for(int i=0;i<4;i++){ f32x4 p=*(const LAS f32x4*)(smem+5*SLOT+(w^1)*4096+i*1024+l*16);
      f32x2 va=(pr(keep,2*i)+f32x2{p.x,p.y})*rv+(un(bv[i][0])+pr(hres,2*i)), vb=(pr(keep,2*i+1)+f32x2{p.z,p.w})*rv+(un(bv[i][1])+pr(hres,2*i+1));
      o[2*i]=pk2(va); o[2*i+1]=pk2(vb); }
    rsrc_t ro_=mk(out, S*D*2);
    #pragma unroll
    for(int k=0;k<2;k++){
      auto r0=__builtin_amdgcn_permlane32_swap(o[4*k],o[4*k+2],false,false);
      auto r1=__builtin_amdgcn_permlane32_swap(o[4*k+1],o[4*k+3],false,false);
      st16(u32x4{r0[0],r1[0],r0[1],r1[1]}, ro_, (tt*32+r)*896+j*128+kh*64+hh*16+k*32);
    }
    STAMP(20);
    STAMP_FLUSH;
  }
}

extern "C" void kernel_launch(void* const* d_in, const int* in_sizes, int n_in, void* d_out, int out_size, void* d_ws, size_t ws_size, hipStream_t stream) {
  (void)hipFuncSetAttribute((const void*)fused, hipFuncAttributeMaxDynamicSharedMemorySize, LDS_BYTES);
  fused<<<256,448,LDS_BYTES,stream>>>((const u16*)d_in[0],(const u16*)d_in[1],(const u16*)d_in[2],(const u16*)d_in[4],(const u16*)d_in[5],(const u16*)d_in[6],(const u16*)d_in[7],(char*)d_ws,(u16*)d_out);
}
```

```cpp
#define THR1 8
#define NW1B 3
#include <hip/hip_runtime.h>
#include <hip/hip_bf16.h>
typedef unsigned short u16;
typedef unsigned u32;
typedef unsigned long long u64;
typedef short s16x4 __attribute__((ext_vector_type(4)));
typedef __bf16 bf16x8 __attribute__((ext_vector_type(8)));
typedef __bf16 bf16x2 __attribute__((ext_vector_type(2)));
typedef float f32x16 __attribute__((ext_vector_type(16)));
typedef float f32x4 __attribute__((ext_vector_type(4)));
typedef float f32x2 __attribute__((ext_vector_type(2)));
typedef unsigned u32x4 __attribute__((ext_vector_type(4)));
typedef unsigned u32x2 __attribute__((ext_vector_type(2)));
#define LAS __attribute__((address_space(3)))
#define DI __device__ __forceinline__
typedef __amdgpu_buffer_rsrc_t rsrc_t;

#define CTOUCH 1
#define WSWARM 1
#define REMAP 1
#define PDK 5
#define PDV 5
#define B3POS 12
#define PUBA 19
#define GPD 9
#define FPD 9
#define RWARM 1
#define SL7 12
#define SL8 7
#define SL10 44
#define SL11 10

constexpr int S=2048, D=448, F=1792;
constexpr u32 WS_EPOCH= 0;
constexpr u32 WS_IB1  = 1024;
constexpr u32 WS_IB2  = WS_IB1 + 8*256;
constexpr u32 WS_IB3  = WS_IB2 + 56*256;
constexpr u32 WS_IB4  = WS_IB3 + 64*256;
constexpr u32 WS_KVT  = 131072;
constexpr u32 WS_AUG  = WS_KVT + 32*1024;
constexpr u32 WS_ST   = WS_AUG + 64*1024;
constexpr u32 WS_PQ   = WS_ST + 64*4096;
constexpr u32 WS_KVP  = WS_PQ + 64*28*1024;
constexpr u32 WS_H    = WS_KVP + 16*2*32*1024;
constexpr u32 WS_H1   = WS_H + 64*28*1024;
constexpr u32 WS_END  = WS_H1 + 64*112*1024;
static_assert(WS_IB4 + 3*64*256 <= WS_KVT, "inbox overflow");

constexpr int SC1=16;
constexpr u32 OOB=0x7ffffff0u;
DI rsrc_t mk(const void* p, u32 n){ return __builtin_amdgcn_make_buffer_rsrc((void*)p, 0, n, 0x00020000); }
DI u32 ld4(rsrc_t r, u32 v, u32 s){ return __builtin_amdgcn_raw_buffer_load_b32(r, v, s, 0); }
DI u32x4 ld16(rsrc_t r, u32 v, u32 s){ return __builtin_amdgcn_raw_buffer_load_b128(r, v, s, 0); }
DI u32x2 ld8(rsrc_t r, u32 v, u32 s){ return __builtin_amdgcn_raw_buffer_load_b64(r, v, s, 0); }
DI u32x4 ld16c(rsrc_t r, u32 v, u32 s){ return __builtin_amdgcn_raw_buffer_load_b128(r, v, s, SC1); }
DI u32x2 ld8c(rsrc_t r, u32 v, u32 s){ return __builtin_amdgcn_raw_buffer_load_b64(r, v, s, SC1); }
DI void st16(u32x4 d, rsrc_t r, u32 v){ __builtin_amdgcn_raw_buffer_store_b128(d, r, v, 0, 0); }
DI void st16c(u32x4 d, rsrc_t r, u32 v){ __builtin_amdgcn_raw_buffer_store_b128(d, r, v, 0, SC1); }
DI void st8c(u32x2 d, rsrc_t r, u32 v){ __builtin_amdgcn_raw_buffer_store_b64(d, r, v, 0, SC1); }
DI void st4c(u32 d, rsrc_t r, u32 v){ __builtin_amdgcn_raw_buffer_store_b32(d, r, v, 0, SC1); }
DI void glds(rsrc_t r, char LAS* dst, u32 v, u32 s){ __builtin_amdgcn_raw_ptr_buffer_load_lds(r, (LAS void*)dst, 16, v, s, 0, 0); }
DI f32x16 mfma(u32x4 a, u32x4 b, f32x16 c){ return __builtin_amdgcn_mfma_f32_32x32x16_bf16(__builtin_bit_cast(bf16x8,a), __builtin_bit_cast(bf16x8,b), c, 0,0,0); }
DI u32x4 trfrag(const char LAS* p0, const char LAS* p1){
  s16x4 a=__builtin_amdgcn_ds_read_tr16_b64_v4i16((LAS s16x4*)p0);
  s16x4 b=__builtin_amdgcn_ds_read_tr16_b64_v4i16((LAS s16x4*)p1);
  return __builtin_bit_cast(u32x4, __builtin_shufflevector(a,b,0,1,2,3,4,5,6,7));
}
template<int V> struct IC{ static constexpr int v=V; };
template<int I,int N,class Fn> DI void sfor(Fn&& f){ if constexpr(I<N){ f(IC<I>{}); sfor<I+1,N>(f); } }
#define SFOR(i,a,b) sfor<a,b>([&](auto ic_##i) __attribute__((always_inline)) { constexpr int i=decltype(ic_##i)::v;
#define SEND_ });
template<class T> DI T pin(T v){ asm volatile("" : "+v"(v)); return v; }
DI u32 pk2(f32x2 v){ return __builtin_bit_cast(u32, __builtin_convertvector(v, bf16x2)); }
DI u32 pk(float a, float b){ return pk2(f32x2{a,b}); }
DI float lo(u32 v){ return __uint_as_float(v<<16); }
DI float hi(u32 v){ return __uint_as_float(v&0xffff0000u); }
DI f32x2 un(u32 v){ return f32x2{lo(v),hi(v)}; }
DI f32x2 pr(const f32x16& a, int i){ return f32x2{a[2*i],a[2*i+1]}; }
DI f32x4 qd(const f32x16& a, int i){ return f32x4{a[4*i],a[4*i+1],a[4*i+2],a[4*i+3]}; }
DI u32x4 pack8(const f32x16& a, int s){ u32x4 r={pk2(pr(a,4*s)),pk2(pr(a,4*s+1)),pk2(pr(a,4*s+2)),pk2(pr(a,4*s+3))}; return r; }
DI float relu(float t){ return __int_as_float(max(__float_as_int(t),0)); }
DI float phie(float t){ const float e=__builtin_amdgcn_fmed3f(__builtin_amdgcn_exp2f(__builtin_fmaf(t,1.44269504089f,-1.44269504089e-6f)),0.f,1.f);
  float m=__int_as_float(max(__float_as_int(t),0x358637bd)); asm("" : "+v"(m)); return m+e; }
DI u32 sq2(f32x2 t){ f32x2 m={relu(t.x),relu(t.y)}; return pk2(m*m); }
DI float dot2(u32 a, u32 b, float c){ return __builtin_amdgcn_fdot2_f32_bf16(__builtin_bit_cast(bf16x2,a),__builtin_bit_cast(bf16x2,b),c,false); }
DI u32x4 norm8(u32x4 v, u32 rlo, u32 rhi, float sh, u32x4 keep){
  u32 o0,o1,o2,o3; float t0,t1,t2,t3,t4,t5,t6,t7;
  asm("v_dot2_f32_bf16 %4, %12, %16, %18\n\tv_dot2_f32_bf16 %5, %12, %17, %18\n\t"
      "v_dot2_f32_bf16 %6, %13, %16, %18\n\tv_dot2_f32_bf16 %7, %13, %17, %18\n\t"
      "v_dot2_f32_bf16 %8, %14, %16, %18\n\tv_dot2_f32_bf16 %9, %14, %17, %18\n\t"
      "v_dot2_f32_bf16 %10, %15, %16, %18\n\tv_dot2_f32_bf16 %11, %15, %17, %18\n\t"
      "v_cvt_pk_bf16_f32 %0, %4, %5\n\tv_cvt_pk_bf16_f32 %1, %6, %7\n\t"
      "v_cvt_pk_bf16_f32 %2, %8, %9\n\tv_cvt_pk_bf16_f32 %3, %10, %11"
      : "=&v"(o0),"=&v"(o1),"=&v"(o2),"=&v"(o3),"=&v"(t0),"=&v"(t1),"=&v"(t2),"=&v"(t3),"=&v"(t4),"=&v"(t5),"=&v"(t6),"=&v"(t7)
      : "v"(v[0]),"v"(v[1]),"v"(v[2]),"v"(v[3]),"v"(rlo),"v"(rhi),"v"(sh),"v"(keep));
  return u32x4{o0,o1,o2,o3};
}
DI float quad_sum(float q){
  q+=__uint_as_float(__builtin_amdgcn_update_dpp(0,__float_as_uint(q),0xB1,0xf,0xf,true));
  q+=__uint_as_float(__builtin_amdgcn_update_dpp(0,__float_as_uint(q),0x4E,0xf,0xf,true)); return q; }
DI float xhalf_sum(float v){ auto r=__builtin_amdgcn_permlane32_swap(__float_as_uint(v),__float_as_uint(v),false,false); return __uint_as_float(r[0])+__uint_as_float(r[1]); }
DI float bcast_hi(float v){ auto r=__builtin_amdgcn_permlane32_swap(__float_as_uint(v),__float_as_uint(v),false,false); return __uint_as_float(r[1]); }
#define BAR()  asm volatile("s_barrier" ::: "memory")
#define BARL() asm volatile("s_waitcnt lgkmcnt(0)\n\ts_barrier" ::: "memory")
#define BARV() asm volatile("s_waitcnt vmcnt(0)\n\ts_barrier" ::: "memory")
#define BARVL() asm volatile("s_waitcnt vmcnt(0) lgkmcnt(0)\n\ts_barrier" ::: "memory")
#define WAITV(n) asm volatile("s_waitcnt vmcnt(%0)" :: "n"(n) : "memory")
#define SLEEP(n) asm volatile("s_sleep %0" :: "n"(n) : "memory")
#define SB() __builtin_amdgcn_sched_barrier(0)

constexpr int LDS_BYTES = 163840;
constexpr u32 CODE_BYTES = 35840;
constexpr int A_K=0, A_V=50176, A_X=100352, P0=0, P1=57344, SLOT=16384;

#define STAMP(i) do{}while(0)
#define STAMP_DECL
#define STAMP_FLUSH
#define STAMP_CLK0
#define STAMP_CLK1

DI u32 img_src(int l, int rowstride){ int row=l>>3, pos=l&7; return row*rowstride + ((pos^(((row>>1)&1)<<2))*16); }
DI int img_rd(int l, int nt){ int hh=l>>5, cb=(l>>4)&1, q=(l&15)>>2, p=l&3; return (4*hh+q)*128 + ((nt^(q>>1))*64) + cb*32 + p*8; }

constexpr int A_FLG=163584;
DI void prog_set(char LAS* smem, int w, u32 v){ asm volatile("" ::: "memory"); *(volatile LAS u32*)(smem+A_FLG+w*4)=v; asm volatile("" ::: "memory"); }
DI u32 prog4(char LAS* smem){ u32x4 v=*(volatile LAS u32x4*)(smem+A_FLG); return min(min(v[0],v[1]),min(v[2],v[3])); }
DI u32 prog2(char LAS* smem, int w){ u32x2 v=*(volatile LAS u32x2*)(smem+A_FLG+w*4); return min(v[0],v[1]); }
DI u32 prog1(char LAS* smem, int w){ return *(volatile LAS u32*)(smem+A_FLG+w*4); }
#define SPIN(expr,target) do{ _Pragma("unroll 1") for(int sp_=0; sp_<(1<<22) && (u32)__builtin_amdgcn_readfirstlane(expr)<(target); sp_++){} asm volatile("" ::: "memory"); }while(0)
#define SPINS(expr,target) do{ _Pragma("unroll 1") for(int sp_=0; sp_<(1<<21) && (u32)__builtin_amdgcn_readfirstlane(expr)<(target); sp_++){ SLEEP(1); } asm volatile("" ::: "memory"); }while(0)

#define STOUCH(o) "s_load_dwordx2 %0, %1, " #o "+60\n\t"
#define STOUCH7(o) STOUCH(o) STOUCH(o+128) STOUCH(o+256) STOUCH(o+384) STOUCH(o+512) STOUCH(o+640) STOUCH(o+768)
DI void stouch(const void* base, int first, int n, u64& sj){
  _Pragma("unroll 1")
  for(int i=0;i<n;i++){ const char* p=(const char*)base+(first+i)*896; asm volatile(STOUCH7(0) : "+s"(sj) : "s"(p) : "memory"); } }

DI void send(char* p, u64 tag){ asm volatile("global_atomic_swap_x2 %0, %1, off sc1" :: "v"(p), "v"(tag) : "memory"); }
#define ISS(f) asm volatile("global_atomic_or_x2 %0, %1, %2, off sc0 sc1\n\ts_sleep 2" : "+v"(f) : "v"(p_), "v"(z_) : "memory")
#define WT(f)  asm volatile("s_waitcnt vmcnt(1)" : "+v"(f) :: "memory")
#define POLL(ptr,act,AFTER) do{ if(act){ char* p_=(ptr); const u64 z_=0; ISS(f0); \
    _Pragma("unroll 1") for(int sp_=0;sp_<20000;sp_++){ ISS(f1); WT(f0); if(__all(f0==tag)) break; ISS(f0); WT(f1); if(__all(f1==tag)) break; } \
    AFTER; } }while(0)

__global__ void __launch_bounds__(512,2) fused(const u16* x, const u16* wqkv, const u16* wo, const u16* w1, const u16* b1, const u16* w2, const u16* b2, char* ws, u16* out){
  extern __shared__ __attribute__((aligned(16))) char smem_[];
  char LAS* smem=(char LAS*)smem_;
  const int b=blockIdx.x, l=threadIdx.x&63;
  u32 cjunk=0;
  { u64 pcv; asm volatile("s_getpc_b64 %0" : "=s"(pcv));
    const u32 li=min((u32)((b>>3)*9+(threadIdx.x*9>>9)), CODE_BYTES/128-1);
    if(CTOUCH) asm volatile("global_load_dword %0, %1, %2" : "=v"(cjunk) : "v"(li*128), "s"(pcv)); }
  const int t=(b&7)*4+((b>>3)&3), j=b>>5;
  const int w=__builtin_amdgcn_readfirstlane(threadIdx.x>>6);
  const int ce=t>>1, ue=(t&1)*7+j;
  const int cf= REMAP? (t>>3)*4+(t&3) : ce, fh= REMAP? (t>>2)&1 : t&1, fc=fh*7+j;
  if(w==6){
    u64 tag=__hip_atomic_load((u64*)(ws+WS_EPOCH), __ATOMIC_RELAXED, __HIP_MEMORY_SCOPE_AGENT);
    STAMP_DECL
    const u32 j4=ld4(mk(wqkv,D*3*D*2), l<37? (b*37+l)*128 : OOB, 0);
    u32 j5=0, j6=0;
    if(WSWARM){
      const u32 ib= j==7? WS_IB1+(t>>2)*256 : l<4? WS_IB2+(j*8+(t>>2))*256 : l<8? WS_IB4+(cf*4+(fc>>2))*256 : l<12? WS_IB4+16384+(ce*4+(ue>>2))*256
                  : l<16? WS_IB4+32768+(ce*4+(ue>>2))*256 : WS_IB3+(t*2+(j>>2))*256;
      if(l<(j==7?4:17)){ const u64 z=0; asm volatile("global_atomic_or_x2 %0, %1, off sc1" :: "v"(ws+ib+(l&3)*64), "v"(z) : "memory"); }
      j5=ld4(mk(ws,WS_END), l<13? 0x100000*l+WS_AUG+60*1024 : OOB, 0);
      j6=ld4(mk(out,S*D*2), l<2? 0x100000*l : OOB, 0); }
    prog_set(smem,6,0); BARL();
    u64 sj=0;
    if(j<7){ stouch(wo, b*2, 2, sj); stouch(w1, b*8, 3, sj); }
    asm volatile("" :: "v"(j4), "v"(j5), "v"(j6), "v"(cjunk));
    tag=pin(tag+1);
    char* p1=pin(ws+WS_IB1+j*256+t*8);
    char* p3=pin(ws+WS_IB3+(t*2+(l&1))*256+j*8);
    u64 f0=0, f1=0;
    BAR(); BAR();
    BAR();
    if(l==0) send(p1, tag);
    asm volatile("s_waitcnt lgkmcnt(0)" : "+s"(sj) :: "memory");
    STAMP(0);
    if(j==7){
      char* p2=pin(ws+WS_IB2+l*256+t*8);
      POLL(ws+WS_IB1+(t>>2)*256+l*8, l<32, STAMP(10); BAR());
      BAR();
      if(l<56) send(p2, tag);
      STAMP(2);
      BAR();
      BAR();
      BAR();
      if(l<16 && t<28) send(ws+WS_IB4+(l*4+(t>>3))*256+(14+(t&7))*8, tag);
      STAMP(3);
    } else {
      SPINS(prog4(smem),4);
      if(l<2) send(p3, tag);
      STAMP(1);
      POLL(ws+WS_IB3+(t*2+(j>>2))*256+l*8, l<7, STAMP(11); prog_set(smem,6,1));
      POLL(ws+WS_IB2+(j*8+(t>>2))*256+l*8, l<32, STAMP(12); BAR());
      char* ps=pin(ws+WS_IB4+(ce*4+(l&3))*256+ue*8);
      char* pp=pin(ws+WS_IB4+(cf*4+(fc>>2))*256+(l<14?l:l+2*(fc&3))*8);
      BAR();
      BAR();
      if(l<4) send(ps, tag);
      STAMP(4);
      POLL(pp, l<16, STAMP(13); BAR());
      if(b==0 && l==0) __hip_atomic_store((u64*)(ws+WS_EPOCH), tag, __ATOMIC_RELAXED, __HIP_MEMORY_SCOPE_AGENT);
      ps=pin(ws+WS_IB4+16384+(cf*4+(l&3))*256+fc*8);
      pp=pin(ws+WS_IB4+16384+(ce*4+(ue>>2))*256+l*8);
      BAR();
      BAR();
      if(l<4) send(ps, tag);
      STAMP(5);
      ps=pin(ps+16384);
      POLL(pp, l<14, STAMP(14); BAR());
      pp=pin(pp+16384);
      BAR();
      if(l<4) send(ps, tag);
      STAMP(6);
      POLL(pp, l<14, STAMP(15); BAR());
    }
    asm volatile("s_waitcnt vmcnt(0)" : "+v"(f0), "+v"(f1) :: "memory");
    STAMP_FLUSH;
    return;
  }
  const u32 s896=img_src(l,896);
  rsrc_t rw2=mk(w2, D*F*2);
  auto w2so=[&](int st, int half){ return (u32)(((7*half+st%7)*128+(st/7)*64)*896+j*128); };
  auto warm_w2=[&](int k0, int k1, int dk){
    const u64 order=0xDC543B8210A796ull;
    u32 acc=0;
    _Pragma("unroll 1")
    for(int k=k0;k<k1;k+=dk){ const int st=(int)(order>>((k>>1)*4))&15, half=k&1; const u32 so=((7*half+st%7)*128+(st/7)*64+(t&3)*16)*896;
      const u32 a=ld4(rw2, l<56? l*128:OOB, so), c=ld4(rw2, l<56? l*128:OOB, so+7168); asm volatile("" :: "v"(acc)); acc=a^c; }
    asm volatile("" :: "v"(acc)); };
  if(w==7){
    BAR();
    u64 sj=0;
    if(j<7) stouch(w1, b*8+3, 5, sj);
    asm volatile("s_waitcnt vmcnt(0)" : "+v"(cjunk) :: "memory");
    if(j==7) return;
    BAR(); BAR(); BAR();
    BAR();
    asm volatile("s_waitcnt lgkmcnt(0)" : "+s"(sj) :: "memory");
    u32x4 rc[48];
    #define TLD(g) _Pragma("unroll") for(int i=0;i<8;i++) rc[(g)*8+i]=ld16(rw2, s896, w2so(9+((g)>>1),(g)&1)+i*8*896);
    BAR();
    SLEEP(SL8); TLD(0)
    BAR();
    TLD(1)
    BAR();
    SLEEP(SL10); TLD(2) TLD(3)
    BAR();
    SLEEP(SL11); TLD(4)
    BAR();
    TLD(5)
    BAR();
    { char LAS* d=smem+3*SLOT+l*16;
      #pragma unroll
      for(int i=0;i<8;i++) *(LAS u32x4*)(d+i*1024)=rc[i];
      BAR();
      #pragma unroll
      for(int i=8;i<16;i++) *(LAS u32x4*)(d+i*1024)=rc[i]; }
    BARL();
    { char LAS* d=smem+7*SLOT+l*16;
      #pragma unroll
      for(int i=0;i<32;i++) *(LAS u32x4*)(d+i*1024)=rc[16+i]; }
    BARL();
    return;
  }
  if(w>=4){
    const int lw=w-4;
    const u32 s3584=img_src(l,3584);
    { rsrc_t rw=mk(wqkv, D*3*D*2);
      u32 pat[7];
      #pragma unroll
      for(int i=0;i<7;i++){ int cc=i*64+l, blk=cc/112, rem=cc%112, q=rem/28, r28=rem%28;
        pat[i]=(16*blk+4*(r28/7)+q)*2688 + (r28%7)*16; }
      auto round=[&](int rn){ const int n=2*rn+lw, part=n>=7, ch=n-7*part; const u32 so=ch*64*2688+(part+1)*896+j*112;
        #pragma unroll
        for(int i=0;i<7;i++) glds(rw, smem+n*7168+i*1024, pat[i], so); };
      const u32 s2688=img_src(l,2688);
      auto qchunk=[&](int ch){
        #pragma unroll
        for(int i=0;i<4;i++) glds(rw, smem+A_X+(ch*8+2*i+lw)*1024, s2688, (ch*64+i*16+lw*8)*2688+j*128); };
      prog_set(smem,w,0); BARL();
      round(0); round(1);
      #pragma unroll 1
      for(int rn=2;rn<7;rn++){ round(rn); WAITV(14); prog_set(smem,w,rn-1); }
      WAITV(7); prog_set(smem,w,6);
      asm volatile("" :: "v"(cjunk));
      if(j<7){
        SPINS(prog4(smem),2);
        #pragma unroll 1
        for(int ch=0;ch<4;ch++) qchunk(ch);
        WAITV(16); BAR();
        #pragma unroll 1
        for(int ch=4;ch<7;ch++) qchunk(ch);
        WAITV(12); BAR();
        WAITV(0); prog_set(smem,w,8);
        BAR();
      } else { WAITV(0); BAR(); BAR(); BAR(); }
    }
    rsrc_t rw1=mk(w1, D*F*2);
    if(j==7){
      BAR(); BAR();
      if(t<28){
        #pragma unroll 4
        for(int i=0;i<28;i++){ int blk=2*i+lw; glds(rw1, smem+P0+blk*1024, s3584, blk*8*3584+t*128); } }
      WAITV(0); BAR();
      return;
    }
    rsrc_t rwo=mk(wo, D*D*2), rx=mk(x, S*D*2);
    auto w2stage=[&](int st, int slot){
      #pragma unroll
      for(int i=0;i<8;i++) glds(rw2, smem+slot*SLOT+lw*8192+i*1024, s896, w2so(st,lw)+i*8*896); };
    #pragma unroll 1
    for(int k=0;k<7;k++){
      #pragma unroll
      for(int i=0;i<4;i++){ const int blk=k*8+2*i+lw; glds(rwo, smem+P0+blk*1024, s896, blk*8*896+j*128); }
      WAITV(THR1); }
    SPINS(prog4(smem),3);
    { const int row=l>>2; const u32 sx=row*896+(((l&3)^(row>>2))*16);
      #pragma unroll
      for(int i=0;i<4;i++) glds(rx, smem+9*SLOT+(2*lw+(i&1))*4096+(i>>1)*1024, sx, (t*64+lw*32+(i>>1)*16)*896+j*128+(i&1)*64); }
    #pragma unroll 1
    for(int k=0;k<NW1B;k++){
      #pragma unroll
      for(int i=0;i<4;i++){ const int blk=k*8+2*i+lw; glds(rw1, smem+P1+blk*1024, s3584, blk*8*3584+fc*256); }
      WAITV(THR1); }
    WAITV(0); BAR();
    SLEEP(SL7);
    #pragma unroll 1
    for(int k=NW1B;k<7;k++){
      #pragma unroll
      for(int i=0;i<4;i++){ const int blk=k*8+2*i+lw; glds(rw1, smem+P1+blk*1024, s3584, blk*8*3584+fc*256); } }
    u32x4 ra[28], rb[24];
    #define LLD(g) _Pragma("unroll") for(int i=0;i<8;i++) rb[(g)*8+i]=ld16(rw2, s896, w2so(6+(g),lw)+i*8*896);
    #pragma unroll
    for(int i=0;i<28;i++) ra[i]=ld16(rw1, s3584, (2*i+lw)*8*3584+fc*256+128);
    LLD(0)
    BAR();
    SLEEP(SL8); LLD(1)
    WAITV(44); BAR();
    w2stage(0,7);
    BAR();
    { char LAS* d=smem+P0+lw*1024+l*16;
      #pragma unroll
      for(int i=0;i<28;i++) *(LAS u32x4*)(d+i*2048)=ra[i]; }
    SLEEP(SL10-14);
    w2stage(1,8); w2stage(2,9);
    BARL();
    SLEEP(SL11);
    LLD(2) w2stage(3,4);
    BAR();
    w2stage(4,5); w2stage(5,6);
    WAITV(16); BAR();
    char LAS* d=smem+lw*8192+l*16;
    #pragma unroll
    for(int i=0;i<8;i++) *(LAS u32x4*)(d+i*1024)=rb[i];
    BARV();
    #pragma unroll
    for(int i=8;i<24;i++) *(LAS u32x4*)(d+(i>>3)*SLOT+(i&7)*1024)=rb[i];
    BARL();
    w2stage(12,9); w2stage(13,4);
    BARV();
    return;
  }
  STAMP_DECL
  STAMP(0); STAMP_CLK0
  rsrc_t rs=mk(ws, WS_END);
  const int th=w>>1, hd=w&1, kh=w&1, hh=l>>5, r=l&31, tt=2*t+th;
  const f32x16 zero={};
  {
    rsrc_t rx=mk(x, S*D*2);
    u32x4 raw[14];
    { const int row=16*w+(l>>2);
      #pragma unroll
      for(int i=0;i<14;i++) raw[i]=ld16(rx, row*896+(l&3)*16+i*64, t*64*896); }
    prog_set(smem,w,0); BARL();
    const char LAS* x0=pin(smem+A_X+(th*56+hh)*512+((r^(2*hh))*16));
    const char LAS* x1=pin(smem+A_X+(th*56+hh)*512+((r^(4+2*hh))*16));
    const int q=(l&15)>>2, p=l&3, cb=(l>>4)&1;
    const char LAS* kb=pin(smem+A_K+q*448+2*hh*112+hd*48+(cb^th)*32+(p&1)*16+(p>>1)*8);
    const char LAS* vb=pin(smem+A_V+q*448+2*hh*112+hd*56+cb*32+p*8);
    const char LAS* qb=pin(smem+A_X+(8*hh+q)*128+((hd^(q>>1))*64)+cb*32+(p&1)*16+(p>>1)*8);
    const u32 kmask=pin(((r>>2)^(th*4))==(hd?0:7)? 0u:0xffffffffu);
    const u32 vmask=pin(r>=28? 0u:0xffffffffu), vone=pin(r==28? 0x3f803f80u:0u);
    char LAS* xs=pin(smem+w*2048+l*16); const char LAS* xr_=pin(smem+(w^2)*2048+l*16);
    const u32 kvo=pin(WS_KVP+(((2*j+hd)*2+th)*32+t)*1024+l*16);
    f32x16 aK=pin(zero+1e-6f);
    { char LAS* xw=pin(smem+A_X+(th*56+p)*512+((((16*w+(l>>2))&31)^(2*p))*16));
      SB();
      float s1=0,s2=0,s1b=0,s2b=0;
      #pragma unroll
      for(int i=0;i<14;i++){
        s1=dot2(raw[i][0],0x3f803f80u,s1); s2=dot2(raw[i][0],raw[i][0],s2); s1b=dot2(raw[i][1],0x3f803f80u,s1b); s2b=dot2(raw[i][1],raw[i][1],s2b);
        s1=dot2(raw[i][2],0x3f803f80u,s1); s2=dot2(raw[i][2],raw[i][2],s2); s1b=dot2(raw[i][3],0x3f803f80u,s1b); s2b=dot2(raw[i][3],raw[i][3],s2b);
        SB(); }
      asm volatile("" :: "v"(cjunk));
      STAMP(1);
      const float m=quad_sum(s1+s1b)*(1.f/448);
      const float var=relu(quad_sum(s2+s2b)*(1.f/448)-m*m);
      const u32 rlo=pk(__builtin_amdgcn_rsqf(var+1e-5f),0.f), rhi=rlo<<16; const float sh=-m*lo(rlo);
      #pragma unroll
      for(int i=0;i<14;i++){ raw[i]=norm8(raw[i],rlo,rhi,sh,raw[i?i-1:1]); *(LAS u32x4*)(xw+i*2048)=raw[i]; SB(); }
    }
    prog_set(smem,w,1);
    STAMP(2);
    SPIN(min(prog2(smem,th*2)*2,prog2(smem,4)),2);
    STAMP(3);
    u32x4 xf[28], fw[84], k0, k1;
    #define XRD(s) xf[s]=*(const LAS u32x4*)((((s)&1)?x1:x0)+(s)*1024)
    #define WRD(n) { if((n)<56){ const char LAS* a_=((n)<28?kb:vb)+((n)%28)*1792; fw[n]=trfrag(a_,a_+112); } else { const char LAS* a_=qb+((n)-56)*2048; fw[n]=trfrag(a_,a_+512); } }
    #define NEED(v) { if((u32)__builtin_amdgcn_readfirstlane(pf)<(v)) SPIN(prog2(smem,4),v); asm volatile("" ::: "memory"); }
    f32x16 aV=zero; u32 pf=0; float ph[16];
    #define RDN(n) ((n)<28-PDK? (n)+PDK : ((n)<28-PDK+(PDV-PDK)? 2*(n)-(28-PDK)+PDK+1 : (n)+PDV))
    #pragma unroll
    for(int n=0;n<PDK;n++){ XRD(n); WRD(n) }
    SB();
    SFOR(n,0,48)
      SFOR(m,(n? RDN(n-1):PDK-1)+1,(RDN(n)<48? RDN(n)+1:48))
        if constexpr(m==13||m==29||m==37) pf=prog2(smem,4);
        if constexpr(m==16) NEED(4)
        if constexpr(m==32) NEED(5)
        if constexpr(m==40) NEED(6)
        if constexpr(m==28) prog_set(smem,w,2);
        if constexpr(m<28) XRD(m);
        WRD(m)
      SEND_
      if constexpr(n==12) STAMP(21);
      if constexpr(n==28) STAMP(22);
      if constexpr(n<28) aK=mfma(xf[n],fw[n],aK); else aV=mfma(xf[n-28],fw[n],aV);
      if constexpr(n>=30 && n<46){ constexpr int e=n-30; ph[e]=phie(aK[e]);
        if constexpr(e&1){ const u32 v=pk(ph[e-1],ph[e])&kmask; if constexpr(e<8) k0[e>>1]=v; else k1[(e>>1)-4]=v; } }
      SB();
    SEND_
    BAR(); STAMP(4);
    SFOR(n,48,55) WRD(n) SEND_
    SB();
    SFOR(n,48,56) if constexpr(n==48) WRD(55) aV=mfma(xf[n-28],fw[n],aV); SB(); SEND_
    { u32x4 v0=pack8(aV,0), v1=pack8(aV,1);
      #pragma unroll
      for(int e=0;e<4;e++){ v0[e]=(v0[e]&vmask)|vone; v1[e]=(v1[e]&vmask)|vone; }
      f32x16 kv=mfma(k0,v0,zero); kv=mfma(k1,v1,kv);
      *(LAS f32x4*)xs=qd(kv,2); *(LAS f32x4*)(xs+1024)=qd(kv,3);
      BARL();
      const f32x4 ka=qd(kv,0)+*(const LAS f32x4*)xr_, kc=qd(kv,1)+*(const LAS f32x4*)(xr_+1024);
      st16c(u32x4{pk(ka.x,ka.y),pk(ka.z,ka.w),pk(kc.x,kc.y),pk(kc.z,kc.w)}, rs, kvo);
    }
    STAMP(5);
    if(j<7){
      f32x16 aQ=zero+1e-6f;
      #pragma unroll
      for(int n=56;n<56+PDV;n++) WRD(n)
      SB();
      #pragma unroll
      for(int n=56;n<84;n++){
        if(n+PDV==69) pf=prog2(smem,4);
        if(n+PDV==72) NEED(8)
        if(n==56+B3POS){ BARV(); STAMP(6); }
        if(n+PDV<84) WRD(n+PDV)
        aQ=mfma(fw[n],xf[n-56],aQ); SB(); }
      #pragma unroll
      for(int g=0;g<2;g++) st16c(u32x4{pk(phie(aQ[8*g]),phie(aQ[8*g+1])),pk(phie(aQ[8*g+2]),phie(aQ[8*g+3])),pk(phie(aQ[8*g+4]),phie(aQ[8*g+5])),pk(phie(aQ[8*g+6]),phie(aQ[8*g+7]))},
                                  rs, WS_PQ+(tt*56+8*j+4*hd+2*g)*512+l*16);
      prog_set(smem,w,3);
      STAMP(7);
      WAITV(0); prog_set(smem,w,4);
    } else { BARV(); }
  }
  if(j==7){
    const u32 clo=pin(0x00003f80u), chi=pin(0x3f800000u), vo=pin((l>>4)*1024+(l&15)*16), so=pin(WS_KVT+t*1024+w*256+(l&15)*16+(l>>4)*4);
    STAMP(8);
    BAR();
    STAMP(9);
    { u32x4 v[8];
      #pragma unroll
      for(int i=0;i<8;i++) v[i]=ld16c(rs, vo, WS_KVP+t*32768+w*256+i*4096);
      SB();
      float a[8]={};
      #pragma unroll
      for(int i=0;i<8;i++){
        #pragma unroll
        for(int e=0;e<4;e++){ a[2*e]=dot2(v[i][e],clo,a[2*e]); a[2*e+1]=dot2(v[i][e],chi,a[2*e+1]); }
        SB(); }
      float bq[4];
      #pragma unroll
      for(int i=0;i<4;i++){ auto sw=__builtin_amdgcn_permlane32_swap(__float_as_uint(a[i]),__float_as_uint(a[i+4]),false,false); bq[i]=__uint_as_float(sw[0])+__uint_as_float(sw[1]); }
      float cq[2];
      #pragma unroll
      for(int i=0;i<2;i++){ auto sw=__builtin_amdgcn_permlane16_swap(__float_as_uint(bq[i]),__float_as_uint(bq[i+2]),false,false); cq[i]=__uint_as_float(sw[0])+__uint_as_float(sw[1]); }
      st4c(pk(cq[0],cq[1]), rs, so);
    }
    STAMP(10);
    BARV();
    STAMP(11);
    LAS float* red=(LAS float*)(smem+P1);
    u32 bb=0;
    if(t<28) bb=b1[t*64+hd*32+r];
    BAR();
    if(t<28){
      const char LAS* a=smem+img_rd(l,hd)+P0+th*14*2048;
      const u32x4 ones={0x3f803f80u,0x3f803f80u,0x3f803f80u,0x3f803f80u};
      u32x4 f[14];
      #pragma unroll
      for(int s=0;s<14;s++) f[s]=trfrag(a+s*2048,a+s*2048+1024);
      SB();
      f32x16 cs=zero;
      #pragma unroll
      for(int s=0;s<14;s++) cs=mfma(ones,f[s],cs);
      red[w*64+l]=cs[0];
    }
    BARL();
    if(t<28 && w<2){
      const float s=red[w*64+r]+red[(w+2)*64+r];
      u32 ch=pk(s,s); float rem=s-lo(ch); u32 cl=pk(rem,0.f);
      u32x4 o={ch,cl,bb|(bb<<16),0u}; if(hh) o=u32x4{0,0,0,0};
      st16c(o, rs, WS_AUG+(t*2+w)*1024+l*16);
    }
    BARV();
    STAMP(12);
    if(RWARM) warm_w2(w,28,4);
    STAMP(13);
    STAMP_FLUSH;
    return;
  }
  f32x16 hres;
  const char LAS* ik=smem+img_rd(l,kh); const char LAS* is=smem+img_rd(l,kh^1);
  {
    rsrc_t rb=mk(b2, D*2);
    u32x2 bv[4];
    #pragma unroll
    for(int gq=0;gq<4;gq++) bv[gq]=ld8(rb, hh*8+gq*16, j*128+kh*64);
    const char LAS* w0=pin(ik+P0+kh*8*28*128); const char LAS* w1_=pin(is+P0+kh*8*28*128);
    char LAS* es=pin(smem+9*SLOT+w*4096+l*16); const char LAS* er=pin(smem+9*SLOT+(w^1)*4096+l*16);
    const u32 ho=pin(WS_H+(tt*28+4*j+2*kh)*1024+l*16), sto=pin(hh? OOB : WS_ST+(tt*14+2*j+kh)*256+r*8), hm=pin(hh? 0u:0xffffffffu);
    SPINS(prog1(smem,6),1);
    STAMP(8);
    u32x4 pq[16], kv[16];
    #pragma unroll
    for(int i=0;i<16;i++) pq[i]=ld16c(rs, l*16+((i>>1)&1)*1536+(i&1)*1024, WS_PQ+(tt*56+kh*28+(i>>2)*7)*512);
    u64 sj=0; stouch(w2, b*8+w*2, 2, sj);
    BAR();
    STAMP(9);
    #pragma unroll
    for(int i=0;i<16;i++) kv[i]=ld16c(rs, l*16+(i&3)*1024, WS_KVT+kh*16384+(i>>2)*4096);
    asm volatile("s_waitcnt lgkmcnt(0)" : "+s"(sj) :: "memory");
    SB();
    f32x16 acc0, acc1=zero;
    #pragma unroll
    for(int gq=0;gq<4;gq++){ u32x2 xr=*(const LAS u32x2*)(smem+9*SLOT+w*4096+r*64+((gq^((r>>2)&3))*16)+hh*8);
      acc0[4*gq]=lo(xr[0]); acc0[4*gq+1]=hi(xr[0]); acc0[4*gq+2]=lo(xr[1]); acc0[4*gq+3]=hi(xr[1]); }
    acc0=pin(acc0);
    SB();
    f32x16 num[8]; u32x4 o0[8], o1[8];
    #define NUM(h) { num[h]=mfma(kv[2*(h)],pq[2*(h)],zero); num[h]=mfma(kv[2*(h)+1],pq[2*(h)+1],num[h]); }
    #define FIN(h) { const float rc=__builtin_amdgcn_rcpf(bcast_hi(num[h][12])+1e-6f); f32x16 n_=num[h]*rc; o0[h]=pack8(n_,0); o1[h]=pack8(n_,1); o1[h][2]&=hm; }
    NUM(0) NUM(1) SB(); FIN(0) SB();
    #pragma unroll
    for(int h=0;h<8;h++){
      const int ro=h*28*128;
      u32x4 fa=trfrag(w0+ro,w0+ro+1024), fb=trfrag(w1_+ro,w1_+ro+1024), fc_=trfrag(w0+ro+2048,w0+ro+3072), fd=trfrag(w1_+ro+2048,w1_+ro+3072);
      if(h+2<8) NUM(h+2)
      SB();
      acc1=mfma(fb,o0[h],acc1); acc1=mfma(fd,o1[h],acc1); acc0=mfma(fa,o0[h],acc0); acc0=mfma(fc_,o1[h],acc0);
      if(h+1<8) FIN(h+1)
      __builtin_amdgcn_sched_group_barrier(0x8,1,0); __builtin_amdgcn_sched_group_barrier(0x402,6,0);
      __builtin_amdgcn_sched_group_barrier(0x8,1,0); __builtin_amdgcn_sched_group_barrier(0x402,6,0);
      __builtin_amdgcn_sched_group_barrier(0x8,1,0); __builtin_amdgcn_sched_group_barrier(0x402,6,0);
      __builtin_amdgcn_sched_group_barrier(0x8,1,0); __builtin_amdgcn_sched_group_barrier(0x402,6,0);
      SB();
    }
    #pragma unroll
    for(int i=0;i<4;i++) *(LAS f32x4*)(es+i*1024)=qd(acc1,i);
    STAMP(10);
    BARL();
    #pragma unroll
    for(int i=0;i<4;i++){ const f32x4 v=qd(acc0,i)+*(const LAS f32x4*)(er+i*1024); acc0[4*i]=v.x; acc0[4*i+1]=v.y; acc0[4*i+2]=v.z; acc0[4*i+3]=v.w; }
    st16c(pack8(acc0,0), rs, ho); st16c(pack8(acc0,1), rs, ho+1024);
    SB();
    f32x2 s1=pr(acc0,0), s2=s1*s1;
    #pragma unroll
    for(int i=1;i<8;i++){ f32x2 va=pr(acc0,i); s1+=va; s2=__builtin_elementwise_fma(va,va,s2); }
    st8c(u32x2{__float_as_uint(xhalf_sum(s1.x+s1.y)),__float_as_uint(xhalf_sum(s2.x+s2.y))}, rs, sto);
    STAMP(11);
    SB();
    #pragma unroll
    for(int i=0;i<4;i++){ f32x2 va=pr(acc0,2*i)+un(bv[i][0]), vb=pr(acc0,2*i+1)+un(bv[i][1]);
      hres[4*i]=va.x; hres[4*i+1]=va.y; hres[4*i+2]=vb.x; hres[4*i+3]=vb.y; }
    hres=pin(hres);
    BARV();
  }
  auto stats=[&](const u32x2 (&st)[7], float& mu, float& var){
    f32x2 v=__builtin_bit_cast(f32x2,st[0]);
    #pragma unroll
    for(int i=1;i<7;i++) v+=__builtin_bit_cast(f32x2,st[i]);
    mu=xhalf_sum(v.x)*(1.f/448); var=relu(xhalf_sum(v.y)*(1.f/448)-mu*mu)+1e-5f; };
  {
    const int tf=4*cf+w;
    const char LAS* i0=smem+img_rd(l,0); const char LAS* i1=smem+img_rd(l,1);
    const char LAS* a0p=pin(i0+P1); const char LAS* a1p=pin(i1+P1); const char LAS* b0p=pin(i0+P0); const char LAS* b1p=pin(i1+P0);
    u32x4 fa[56], fb[56];
    #define RDA(s) { const char LAS* a_=((s)<28?a0p:b0p)+((s)%28)*2048; fa[s]=trfrag(a_,a_+1024); }
    #define RDB(s) { const char LAS* a_=((s)<28?a1p:b1p)+((s)%28)*2048; fb[s]=trfrag(a_,a_+1024); }
    RDA(0) RDB(0) RDA(1) RDB(1) RDA(2) RDB(2)
    BAR();
    STAMP(12);
    u32x4 hf_[28], aug[4]; u32x2 st[7];
    #define LDF(s) hf_[s]=ld16c(rs, l*16+((s)&3)*1024, WS_H+(tf*28+((s)&~3))*1024)
    #pragma unroll
    for(int s=0;s<FPD;s++) LDF(s);
    SB();
    f32x16 a0=zero, a1=zero;
    u32x4 bs;
    #pragma unroll
    for(int s=0;s<28;s++){
      if(s==25){ BAR(); }
      if(s+FPD<28) LDF(s+FPD);
      RDA(s+3)
      a0=mfma(fa[s],hf_[s],a0); SB();
      if(s>=5 && s<12) st[s-5]=ld8c(rs, r*8+hh*1792+(s-5)*256, WS_ST+tf*3584);
      if(s>=14 && s<18) aug[s-14]=ld16c(rs, hh? OOB : l*16+(s-14)*1024, WS_AUG+fc*4096);
      RDB(s+3)
      a1=mfma(fb[s],hf_[s],a1);
      if(s==22){ float mu,var; stats(st,mu,var);
        const float sg=__builtin_amdgcn_sqrtf(var); u32 mh=pk(-mu,-mu); float mr=-mu-lo(mh); u32 sh=pk(sg,sg); float sr=sg-lo(sh);
        bs=u32x4{pk(lo(mh),mr), mh, pk(lo(sh),sr), 0u}; }
      SB();
    }
    STAMP(13);
    a0=mfma(aug[0],bs,a0); a1=mfma(aug[1],bs,a1);
    f32x16 b0=mfma(aug[2],bs,zero), b1_=mfma(aug[3],bs,zero);
    SB();
    const u32 ho=WS_H1+(tf*112+fh*56+j*4)*1024+l*16;
    u32x4 pa;
    #pragma unroll
    for(int s=0;s<28;s++){
      if(s+3<28) RDA(28+s+3)
      b0=mfma(fa[28+s],hf_[s],b0);
      if(s<8){ const int e=2*s; pa[e&3]=sq2(pr(s<4?a0:a1,e&7)); }
      SB();
      if(s+3<28) RDB(28+s+3)
      b1_=mfma(fb[28+s],hf_[s],b1_);
      if(s<8){ const int e=2*s+1; pa[e&3]=sq2(pr(s<4?a0:a1,e&7)); if(s&1) st16c(pa, rs, ho+(s>>1)*1024); }
      SB();
      if(s==PUBA){ BARV(); }
    }
    STAMP(14);
    #pragma unroll
    for(int k=0;k<4;k++){ const f32x16& a=k<2?b0:b1_; const int e=(k&1)*4;
      st16c(u32x4{sq2(pr(a,e)),sq2(pr(a,e+1)),sq2(pr(a,e+2)),sq2(pr(a,e+3))}, rs, ho+28672+k*1024); }
    STAMP(15);
  }
  {
    const u32 hb=WS_H1+(tt*112+kh*56)*1024;
    u32x4 a[56], fa[56], fb[56]; u32x2 st[7];
    const char LAS* g0=pin(ik+kh*8192); const char LAS* g1=pin(is+kh*8192);
    const char LAS* g0h=pin(g0+4*SLOT); const char LAS* g1h=pin(g1+4*SLOT);
    const char LAS* g0x=pin(g0+8*SLOT); const char LAS* g1x=pin(g1+8*SLOT);
    constexpr int slot[14]={7,8,9,4,5,6,0,1,2,3,7,8,9,4};
    #define GADDR(bl,bh,bx,s) ((slot[(s)>>2]<4?(bl):(slot[(s)>>2]<8?(bh):(bx)))+(slot[(s)>>2]&3)*SLOT+((s)&3)*2048)
    #define GRA(s) fa[s]=trfrag(GADDR(g0,g0h,g0x,s),GADDR(g0,g0h,g0x,s)+1024)
    #define GRB(s) fb[s]=trfrag(GADDR(g1,g1h,g1x,s),GADDR(g1,g1h,g1x,s)+1024)
    #define LDH(s) a[s]=ld16c(rs, l*16+((s)&3)*1024, hb+((s)&~3)*1024)
    BAR();
    STAMP(16);
    #pragma unroll
    for(int s=0;s<GPD;s++) LDH(s);
    GRA(0); GRB(0); GRA(1); GRB(1); GRA(2); GRB(2);
    WAITV(GPD); BAR();
    SB();
    f32x16 acc0=zero, acc1=zero; float rv;
    #pragma unroll
    for(int s=0;s<56;s++){
      if(s==28-GPD){ STAMP(17); BAR(); STAMP(18); }
      if(s==37){ BAR(); }
      if(s+GPD<56) LDH(s+GPD);
      if(s+3<56) GRB(s+3);
      acc1=mfma(fb[s],a[s],acc1); SB();
      if(s>=38 && s<45) st[s-38]=ld8c(rs, r*8+hh*1792+(s-38)*256, WS_ST+tt*3584);
      if(s+3<56) GRA(s+3);
      acc0=mfma(fa[s],a[s],acc0);
      if(s==53){ float mu,var; stats(st,mu,var); rv=pin(__builtin_amdgcn_rcpf(var)); }
      SB();
    }
    STAMP(19);
    #pragma unroll
    for(int i=0;i<4;i++) *(LAS f32x4*)(smem+5*SLOT+w*4096+i*1024+l*16)=qd(acc1,i);
    BARL();
    u32 o[8];
    #pragma unroll
    for(int i=0;i<4;i++){ const f32x4 v=(qd(acc0,i)+*(const LAS f32x4*)(smem+5*SLOT+(w^1)*4096+i*1024+l*16))*rv+qd(hres,i);
      o[2*i]=pk(v.x,v.y); o[2*i+1]=pk(v.z,v.w); }
    rsrc_t ro_=mk(out, S*D*2);
    #pragma unroll
    for(int k=0;k<2;k++){
      auto r0=__builtin_amdgcn_permlane32_swap(o[4*k],o[4*k+2],false,false);
      auto r1=__builtin_amdgcn_permlane32_swap(o[4*k+1],o[4*k+3],false,false);
      st16(u32x4{r0[0],r1[0],r0[1],r1[1]}, ro_, (tt*32+r)*896+j*128+kh*64+hh*16+k*32);
    }
    STAMP(20); STAMP_CLK1
    STAMP_FLUSH;
  }
}

extern "C" void kernel_launch(void* const* d_in, const int* in_sizes, int n_in, void* d_out, int out_size, void* d_ws, size_t ws_size, hipStream_t stream) {
  (void)hipFuncSetAttribute((const void*)fused, hipFuncAttributeMaxDynamicSharedMemorySize, LDS_BYTES);
  fused<<<256,512,LDS_BYTES,stream>>>((const u16*)d_in[0],(const u16*)d_in[1],(const u16*)d_in[2],(const u16*)d_in[4],(const u16*)d_in[5],(const u16*)d_in[6],(const u16*)d_in[7],(char*)d_ws+0,(u16*)d_out);
}
```

```cpp
#include <hip/hip_runtime.h>
#include <hip/hip_bf16.h>
typedef unsigned short u16;
typedef unsigned u32;
typedef unsigned long long u64;
typedef short s16x4 __attribute__((ext_vector_type(4)));
typedef __bf16 bf16x8 __attribute__((ext_vector_type(8)));
typedef __bf16 bf16x2 __attribute__((ext_vector_type(2)));
typedef float f32x16 __attribute__((ext_vector_type(16)));
typedef float f32x4 __attribute__((ext_vector_type(4)));
typedef float f32x2 __attribute__((ext_vector_type(2)));
typedef unsigned u32x4 __attribute__((ext_vector_type(4)));
typedef unsigned u32x2 __attribute__((ext_vector_type(2)));
#define LAS __attribute__((address_space(3)))
#define DI __device__ __forceinline__
typedef __amdgpu_buffer_rsrc_t rsrc_t;

#define CTOUCH 1
#define REMAP 1
#define PDK 5
#define PDV 5
#define B3POS 12
#define PUBA 19
#define GPD 9
#define FPD 9
#define RWARM 1
#define SL7 12
#define SL8 7
#define SL10 44
#define SL11 10

constexpr int S=2048, D=448, F=1792;
constexpr u32 WS_EPOCH= 0;
constexpr u32 WS_IB1  = 1024;
constexpr u32 WS_IB2  = WS_IB1 + 8*256;
constexpr u32 WS_IB3  = WS_IB2 + 56*256;
constexpr u32 WS_IB4  = WS_IB3 + 64*256;
constexpr u32 WS_KVT  = 131072;
constexpr u32 WS_AUG  = WS_KVT + 32*1024;
constexpr u32 WS_ST   = WS_AUG + 64*1024;
constexpr u32 WS_PQ   = WS_ST + 64*4096;
constexpr u32 WS_KVP  = WS_PQ + 64*28*1024;
constexpr u32 WS_H    = WS_KVP + 16*2*32*1024;
constexpr u32 WS_H1   = WS_H + 64*28*1024;
constexpr u32 WS_END  = WS_H1 + 64*112*1024;
static_assert(WS_IB4 + 3*64*256 <= WS_KVT, "inbox overflow");

constexpr int SC1=16;
constexpr u32 OOB=0x7ffffff0u;
DI rsrc_t mk(const void* p, u32 n){ return __builtin_amdgcn_make_buffer_rsrc((void*)p, 0, n, 0x00020000); }
DI u32 ld4(rsrc_t r, u32 v, u32 s){ return __builtin_amdgcn_raw_buffer_load_b32(r, v, s, 0); }
DI u32x4 ld16(rsrc_t r, u32 v, u32 s){ return __builtin_amdgcn_raw_buffer_load_b128(r, v, s, 0); }
DI u32x2 ld8(rsrc_t r, u32 v, u32 s){ return __builtin_amdgcn_raw_buffer_load_b64(r, v, s, 0); }
DI u32x4 ld16c(rsrc_t r, u32 v, u32 s){ return __builtin_amdgcn_raw_buffer_load_b128(r, v, s, SC1); }
DI u32x2 ld8c(rsrc_t r, u32 v, u32 s){ return __builtin_amdgcn_raw_buffer_load_b64(r, v, s, SC1); }
DI void st16(u32x4 d, rsrc_t r, u32 v){ __builtin_amdgcn_raw_buffer_store_b128(d, r, v, 0, 0); }
DI void st16c(u32x4 d, rsrc_t r, u32 v){ __builtin_amdgcn_raw_buffer_store_b128(d, r, v, 0, SC1); }
DI void st8c(u32x2 d, rsrc_t r, u32 v){ __builtin_amdgcn_raw_buffer_store_b64(d, r, v, 0, SC1); }
DI void st4c(u32 d, rsrc_t r, u32 v){ __builtin_amdgcn_raw_buffer_store_b32(d, r, v, 0, SC1); }
DI void glds(rsrc_t r, char LAS* dst, u32 v, u32 s){ __builtin_amdgcn_raw_ptr_buffer_load_lds(r, (LAS void*)dst, 16, v, s, 0, 0); }
DI f32x16 mfma(u32x4 a, u32x4 b, f32x16 c){ return __builtin_amdgcn_mfma_f32_32x32x16_bf16(__builtin_bit_cast(bf16x8,a), __builtin_bit_cast(bf16x8,b), c, 0,0,0); }
DI u32x4 trfrag(const char LAS* p0, const char LAS* p1){
  s16x4 a=__builtin_amdgcn_ds_read_tr16_b64_v4i16((LAS s16x4*)p0);
  s16x4 b=__builtin_amdgcn_ds_read_tr16_b64_v4i16((LAS s16x4*)p1);
  return __builtin_bit_cast(u32x4, __builtin_shufflevector(a,b,0,1,2,3,4,5,6,7));
}
template<int V> struct IC{ static constexpr int v=V; };
template<int I,int N,class Fn> DI void sfor(Fn&& f){ if constexpr(I<N){ f(IC<I>{}); sfor<I+1,N>(f); } }
#define SFOR(i,a,b) sfor<a,b>([&](auto ic_##i) __attribute__((always_inline)) { constexpr int i=decltype(ic_##i)::v;
#define SEND_ });
template<class T> DI T pin(T v){ asm volatile("" : "+v"(v)); return v; }
DI u32 pk2(f32x2 v){ return __builtin_bit_cast(u32, __builtin_convertvector(v, bf16x2)); }
DI u32 pk(float a, float b){ return pk2(f32x2{a,b}); }
DI float lo(u32 v){ return __uint_as_float(v<<16); }
DI float hi(u32 v){ return __uint_as_float(v&0xffff0000u); }
DI f32x2 un(u32 v){ return f32x2{lo(v),hi(v)}; }
DI f32x2 pr(const f32x16& a, int i){ return f32x2{a[2*i],a[2*i+1]}; }
DI f32x4 qd(const f32x16& a, int i){ return f32x4{a[4*i],a[4*i+1],a[4*i+2],a[4*i+3]}; }
DI u32x4 pack8(const f32x16& a, int s){ u32x4 r={pk2(pr(a,4*s)),pk2(pr(a,4*s+1)),pk2(pr(a,4*s+2)),pk2(pr(a,4*s+3))}; return r; }
DI float relu(float t){ return __int_as_float(max(__float_as_int(t),0)); }
DI float phie(float t){ const float e=__builtin_amdgcn_fmed3f(__builtin_amdgcn_exp2f(__builtin_fmaf(t,1.44269504089f,-1.44269504089e-6f)),0.f,1.f);
  float m=__int_as_float(max(__float_as_int(t),0x358637bd)); asm("" : "+v"(m)); return m+e; }
DI u32 sq2(f32x2 t){ f32x2 m={relu(t.x),relu(t.y)}; return pk2(m*m); }
DI float dot2(u32 a, u32 b, float c){ return __builtin_amdgcn_fdot2_f32_bf16(__builtin_bit_cast(bf16x2,a),__builtin_bit_cast(bf16x2,b),c,false); }
DI u32x4 norm8(u32x4 v, u32 rlo, u32 rhi, float sh, u32x4 keep){
  u32 o0,o1,o2,o3; float t0,t1,t2,t3,t4,t5,t6,t7;
  asm("v_dot2_f32_bf16 %4, %12, %16, %18\n\tv_dot2_f32_bf16 %5, %12, %17, %18\n\t"
      "v_dot2_f32_bf16 %6, %13, %16, %18\n\tv_dot2_f32_bf16 %7, %13, %17, %18\n\t"
      "v_dot2_f32_bf16 %8, %14, %16, %18\n\tv_dot2_f32_bf16 %9, %14, %17, %18\n\t"
      "v_dot2_f32_bf16 %10, %15, %16, %18\n\tv_dot2_f32_bf16 %11, %15, %17, %18\n\t"
      "v_cvt_pk_bf16_f32 %0, %4, %5\n\tv_cvt_pk_bf16_f32 %1, %6, %7\n\t"
      "v_cvt_pk_bf16_f32 %2, %8, %9\n\tv_cvt_pk_bf16_f32 %3, %10, %11"
      : "=&v"(o0),"=&v"(o1),"=&v"(o2),"=&v"(o3),"=&v"(t0),"=&v"(t1),"=&v"(t2),"=&v"(t3),"=&v"(t4),"=&v"(t5),"=&v"(t6),"=&v"(t7)
      : "v"(v[0]),"v"(v[1]),"v"(v[2]),"v"(v[3]),"v"(rlo),"v"(rhi),"v"(sh),"v"(keep));
  return u32x4{o0,o1,o2,o3};
}
DI float quad_sum(float q){
  q+=__uint_as_float(__builtin_amdgcn_update_dpp(0,__float_as_uint(q),0xB1,0xf,0xf,true));
  q+=__uint_as_float(__builtin_amdgcn_update_dpp(0,__float_as_uint(q),0x4E,0xf,0xf,true)); return q; }
DI float xhalf_sum(float v){ auto r=__builtin_amdgcn_permlane32_swap(__float_as_uint(v),__float_as_uint(v),false,false); return __uint_as_float(r[0])+__uint_as_float(r[1]); }
DI float bcast_hi(float v){ auto r=__builtin_amdgcn_permlane32_swap(__float_as_uint(v),__float_as_uint(v),false,false); return __uint_as_float(r[1]); }
#define BAR()  asm volatile("s_barrier" ::: "memory")
#define BARL() asm volatile("s_waitcnt lgkmcnt(0)\n\ts_barrier" ::: "memory")
#define BARV() asm volatile("s_waitcnt vmcnt(0)\n\ts_barrier" ::: "memory")
#define BARVL() asm volatile("s_waitcnt vmcnt(0) lgkmcnt(0)\n\ts_barrier" ::: "memory")
#define WAITV(n) asm volatile("s_waitcnt vmcnt(%0)" :: "n"(n) : "memory")
#define SLEEP(n) asm volatile("s_sleep %0" :: "n"(n) : "memory")
#define SB() __builtin_amdgcn_sched_barrier(0)

constexpr int LDS_BYTES = 163840;
constexpr u32 CODE_BYTES = 35840;
constexpr int A_K=0, A_V=50176, A_X=100352, P0=0, P1=57344, SLOT=16384;

#define STAMP(i) do{}while(0)
#define STAMP_DECL
#define STAMP_FLUSH
#define STAMP_CLK0
#define STAMP_CLK1

DI u32 img_src(int l, int rowstride){ int row=l>>3, pos=l&7; return row*rowstride + ((pos^(((row>>1)&1)<<2))*16); }
DI int img_rd(int l, int nt){ int hh=l>>5, cb=(l>>4)&1, q=(l&15)>>2, p=l&3; return (4*hh+q)*128 + ((nt^(q>>1))*64) + cb*32 + p*8; }

constexpr int A_FLG=163584;
DI void prog_set(char LAS* smem, int w, u32 v){ asm volatile("" ::: "memory"); *(volatile LAS u32*)(smem+A_FLG+w*4)=v; asm volatile("" ::: "memory"); }
DI u32 prog4(char LAS* smem){ u32x4 v=*(volatile LAS u32x4*)(smem+A_FLG); return min(min(v[0],v[1]),min(v[2],v[3])); }
DI u32 prog2(char LAS* smem, int w){ u32x2 v=*(volatile LAS u32x2*)(smem+A_FLG+w*4); return min(v[0],v[1]); }
DI u32 prog1(char LAS* smem, int w){ return *(volatile LAS u32*)(smem+A_FLG+w*4); }
#define SPIN(expr,target) do{ _Pragma("unroll 1") for(int sp_=0; sp_<(1<<22) && (u32)__builtin_amdgcn_readfirstlane(expr)<(target); sp_++){} asm volatile("" ::: "memory"); }while(0)
#define SPINS(expr,target) do{ _Pragma("unroll 1") for(int sp_=0; sp_<(1<<21) && (u32)__builtin_amdgcn_readfirstlane(expr)<(target); sp_++){ SLEEP(1); } asm volatile("" ::: "memory"); }while(0)

#define STOUCH(o) "s_load_dwordx2 %0, %1, " #o "+60\n\t"
#define STOUCH7(o) STOUCH(o) STOUCH(o+128) STOUCH(o+256) STOUCH(o+384) STOUCH(o+512) STOUCH(o+640) STOUCH(o+768)
DI void stouch(const void* base, int first, int n, u64& sj){
  _Pragma("unroll 1")
  for(int i=0;i<n;i++){ const char* p=(const char*)base+(first+i)*896; asm volatile(STOUCH7(0) : "+s"(sj) : "s"(p) : "memory"); } }

DI void send(char* p, u64 tag){ asm volatile("global_atomic_swap_x2 %0, %1, off sc1" :: "v"(p), "v"(tag) : "memory"); }
#define ISS(f) asm volatile("global_atomic_or_x2 %0, %1, %2, off sc0 sc1\n\ts_sleep 2" : "+v"(f) : "v"(p_), "v"(z_) : "memory")
#define WT(f)  asm volatile("s_waitcnt vmcnt(1)" : "+v"(f) :: "memory")
#define POLL(ptr,act,AFTER) do{ if(act){ char* p_=(ptr); const u64 z_=0; ISS(f0); \
    _Pragma("unroll 1") for(int sp_=0;sp_<20000;sp_++){ ISS(f1); WT(f0); if(__all(f0==tag)) break; ISS(f0); WT(f1); if(__all(f1==tag)) break; } \
    AFTER; } }while(0)

__global__ void __launch_bounds__(512,2) fused(const u16* x, const u16* wqkv, const u16* wo, const u16* w1, const u16* b1, const u16* w2, const u16* b2, char* ws, u16* out){
  extern __shared__ __attribute__((aligned(16))) char smem_[];
  char LAS* smem=(char LAS*)smem_;
  const int b=blockIdx.x, l=threadIdx.x&63;
  u32 cjunk=0;
  { u64 pcv; asm volatile("s_getpc_b64 %0" : "=s"(pcv));
    const u32 li=min((u32)((b>>3)*9+(threadIdx.x*9>>9)), CODE_BYTES/128-1);
    if(CTOUCH) asm volatile("global_load_dword %0, %1, %2" : "=v"(cjunk) : "v"(li*128), "s"(pcv)); }
  const int t=(b&7)*4+((b>>3)&3), j=b>>5;
  const int w=__builtin_amdgcn_readfirstlane(threadIdx.x>>6);
  const int ce=t>>1, ue=(t&1)*7+j;
  const int cf= REMAP? (t>>3)*4+(t&3) : ce, fh= REMAP? (t>>2)&1 : t&1, fc=fh*7+j;
  if(w==6){
    u64 tag=__hip_atomic_load((u64*)(ws+WS_EPOCH), __ATOMIC_RELAXED, __HIP_MEMORY_SCOPE_AGENT);
    STAMP_DECL
    const u32 j4=ld4(mk(wqkv,D*3*D*2), l<37? (b*37+l)*128 : OOB, 0);
    prog_set(smem,6,0); BARL();
    u64 sj=0;
    stouch(wo, b<224? b*2:0, 2, sj); stouch(w1, b*7, 4, sj);
    asm volatile("" :: "v"(j4), "v"(cjunk));
    tag=pin(tag+1);
    char* p1=pin(ws+WS_IB1+j*256+t*8);
    char* p3=pin(ws+WS_IB3+(t*2+(l&1))*256+j*8);
    u64 f0=0, f1=0;
    BAR(); BAR();
    BAR();
    if(l==0) send(p1, tag);
    asm volatile("s_waitcnt lgkmcnt(0)" : "+s"(sj) :: "memory");
    STAMP(0);
    if(j==7){
      char* p2=pin(ws+WS_IB2+l*256+t*8);
      POLL(ws+WS_IB1+(t>>2)*256+l*8, l<32, STAMP(10); BAR());
      BAR();
      if(l<56) send(p2, tag);
      STAMP(2);
      BAR();
      BAR();
      BAR();
      if(l<16 && t<28) send(ws+WS_IB4+(l*4+(t>>3))*256+(14+(t&7))*8, tag);
      STAMP(3);
    } else {
      SPINS(prog4(smem),4);
      if(l<2) send(p3, tag);
      STAMP(1);
      POLL(ws+WS_IB3+(t*2+(j>>2))*256+l*8, l<7, STAMP(11); prog_set(smem,6,1));
      POLL(ws+WS_IB2+(j*8+(t>>2))*256+l*8, l<32, STAMP(12); BAR());
      char* ps=pin(ws+WS_IB4+(ce*4+(l&3))*256+ue*8);
      char* pp=pin(ws+WS_IB4+(cf*4+(fc>>2))*256+(l<14?l:l+2*(fc&3))*8);
      BAR();
      BAR();
      if(l<4) send(ps, tag);
      STAMP(4);
      POLL(pp, l<16, STAMP(13); BAR());
      if(b==0 && l==0) __hip_atomic_store((u64*)(ws+WS_EPOCH), tag, __ATOMIC_RELAXED, __HIP_MEMORY_SCOPE_AGENT);
      ps=pin(ws+WS_IB4+16384+(cf*4+(l&3))*256+fc*8);
      pp=pin(ws+WS_IB4+16384+(ce*4+(ue>>2))*256+l*8);
      BAR();
      BAR();
      if(l<4) send(ps, tag);
      STAMP(5);
      ps=pin(ps+16384);
      POLL(pp, l<14, STAMP(14); BAR());
      pp=pin(pp+16384);
      BAR();
      if(l<4) send(ps, tag);
      STAMP(6);
      POLL(pp, l<14, STAMP(15); BAR());
    }
    asm volatile("s_waitcnt vmcnt(0)" : "+v"(f0), "+v"(f1) :: "memory");
    STAMP_FLUSH;
    return;
  }
  const u32 s896=img_src(l,896);
  rsrc_t rw2=mk(w2, D*F*2);
  auto w2so=[&](int st, int half){ return (u32)(((7*half+st%7)*128+(st/7)*64)*896+j*128); };
  auto warm_w2=[&](int k0, int k1, int dk){
    const u64 order=0xDC543B8210A796ull;
    u32 acc=0;
    _Pragma("unroll 1")
    for(int k=k0;k<k1;k+=dk){ const int st=(int)(order>>((k>>1)*4))&15, half=k&1; const u32 so=((7*half+st%7)*128+(st/7)*64+(t&3)*16)*896;
      const u32 a=ld4(rw2, l<56? l*128:OOB, so), c=ld4(rw2, l<56? l*128:OOB, so+7168); asm volatile("" :: "v"(acc)); acc=a^c; }
    asm volatile("" :: "v"(acc)); };
  if(w==7){
    BAR();
    u64 sj=0;
    stouch(w1, b*7+4, 3, sj); stouch(w2, b*3, 3, sj);
    asm volatile("s_waitcnt vmcnt(0)" : "+v"(cjunk) :: "memory");
    if(j==7){ asm volatile("s_waitcnt lgkmcnt(0)" : "+s"(sj) :: "memory"); return; }
    BAR(); BAR(); BAR();
    stouch(w2, min(768+5*b,1787), 5, sj);
    BAR();
    asm volatile("s_waitcnt lgkmcnt(0)" : "+s"(sj) :: "memory");
    u32x4 rc[48];
    #define TLD(g) _Pragma("unroll") for(int i=0;i<8;i++) rc[(g)*8+i]=ld16(rw2, s896, w2so(9+((g)>>1),(g)&1)+i*8*896);
    BAR();
    SLEEP(SL8); TLD(0)
    BAR();
    TLD(1)
    BAR();
    SLEEP(SL10); TLD(2) TLD(3)
    BAR();
    SLEEP(SL11); TLD(4)
    BAR();
    TLD(5)
    BAR();
    { char LAS* d=smem+3*SLOT+l*16;
      #pragma unroll
      for(int i=0;i<8;i++) *(LAS u32x4*)(d+i*1024)=rc[i];
      BAR();
      #pragma unroll
      for(int i=8;i<16;i++) *(LAS u32x4*)(d+i*1024)=rc[i]; }
    BARL();
    { char LAS* d=smem+7*SLOT+l*16;
      #pragma unroll
      for(int i=0;i<32;i++) *(LAS u32x4*)(d+i*1024)=rc[16+i]; }
    BARL();
    return;
  }
  if(w>=4){
    const int lw=w-4;
    const u32 s3584=img_src(l,3584);
    { rsrc_t rw=mk(wqkv, D*3*D*2);
      u32 pat[7];
      #pragma unroll
      for(int i=0;i<7;i++){ int cc=i*64+l, blk=cc/112, rem=cc%112, q=rem/28, r28=rem%28;
        pat[i]=(16*blk+4*(r28/7)+q)*2688 + (r28%7)*16; }
      auto round=[&](int rn){ const int n=2*rn+lw, part=n>=7, ch=n-7*part; const u32 so=ch*64*2688+(part+1)*896+j*112;
        #pragma unroll
        for(int i=0;i<7;i++) glds(rw, smem+n*7168+i*1024, pat[i], so); };
      const u32 s2688=img_src(l,2688);
      auto qchunk=[&](int ch){
        #pragma unroll
        for(int i=0;i<4;i++) glds(rw, smem+A_X+(ch*8+2*i+lw)*1024, s2688, (ch*64+i*16+lw*8)*2688+j*128); };
      prog_set(smem,w,0); BARL();
      round(0); round(1);
      #pragma unroll 1
      for(int rn=2;rn<7;rn++){ round(rn); WAITV(14); prog_set(smem,w,rn-1); }
      WAITV(7); prog_set(smem,w,6);
      asm volatile("" :: "v"(cjunk));
      if(j<7){
        SPINS(prog4(smem),2);
        #pragma unroll 1
        for(int ch=0;ch<4;ch++) qchunk(ch);
        WAITV(16); BAR();
        #pragma unroll 1
        for(int ch=4;ch<7;ch++) qchunk(ch);
        WAITV(12); BAR();
        WAITV(0); prog_set(smem,w,8);
        BAR();
      } else { WAITV(0); BAR(); BAR(); BAR(); }
    }
    rsrc_t rw1=mk(w1, D*F*2);
    if(j==7){
      BAR(); BAR();
      if(t<28){
        #pragma unroll 4
        for(int i=0;i<28;i++){ int blk=2*i+lw; glds(rw1, smem+P0+blk*1024, s3584, blk*8*3584+t*128); } }
      WAITV(0); BAR();
      return;
    }
    rsrc_t rwo=mk(wo, D*D*2), rx=mk(x, S*D*2);
    auto w2stage=[&](int st, int slot){
      #pragma unroll
      for(int i=0;i<8;i++) glds(rw2, smem+slot*SLOT+lw*8192+i*1024, s896, w2so(st,lw)+i*8*896); };
    #pragma unroll 1
    for(int k=0;k<7;k++){
      #pragma unroll
      for(int i=0;i<4;i++){ const int blk=k*8+2*i+lw; glds(rwo, smem+P0+blk*1024, s896, blk*8*896+j*128); } }
    SPINS(prog4(smem),3);
    { const int row=l>>2; const u32 sx=row*896+(((l&3)^(row>>2))*16);
      #pragma unroll
      for(int i=0;i<4;i++) glds(rx, smem+9*SLOT+(2*lw+(i&1))*4096+(i>>1)*1024, sx, (t*64+lw*32+(i>>1)*16)*896+j*128+(i&1)*64); }
    #pragma unroll 1
    for(int k=0;k<7;k++){
      #pragma unroll
      for(int i=0;i<4;i++){ const int blk=k*8+2*i+lw; glds(rw1, smem+P1+blk*1024, s3584, blk*8*3584+fc*256); } }
    WAITV(0); BAR();
    SLEEP(SL7);
    u32x4 ra[28], rb[24];
    #define LLD(g) _Pragma("unroll") for(int i=0;i<8;i++) rb[(g)*8+i]=ld16(rw2, s896, w2so(6+(g),lw)+i*8*896);
    #pragma unroll
    for(int i=0;i<28;i++) ra[i]=ld16(rw1, s3584, (2*i+lw)*8*3584+fc*256+128);
    LLD(0)
    BAR();
    SLEEP(SL8); LLD(1)
    BAR();
    w2stage(0,7);
    BAR();
    { char LAS* d=smem+P0+lw*1024+l*16;
      #pragma unroll
      for(int i=0;i<28;i++) *(LAS u32x4*)(d+i*2048)=ra[i]; }
    SLEEP(SL10-14);
    w2stage(1,8); w2stage(2,9);
    BARL();
    SLEEP(SL11);
    LLD(2) w2stage(3,4);
    BAR();
    w2stage(4,5); w2stage(5,6);
    WAITV(16); BAR();
    char LAS* d=smem+lw*8192+l*16;
    #pragma unroll
    for(int i=0;i<8;i++) *(LAS u32x4*)(d+i*1024)=rb[i];
    BARV();
    #pragma unroll
    for(int i=8;i<24;i++) *(LAS u32x4*)(d+(i>>3)*SLOT+(i&7)*1024)=rb[i];
    BARL();
    w2stage(12,9); w2stage(13,4);
    BARV();
    return;
  }
  STAMP_DECL
  STAMP(0); STAMP_CLK0
  rsrc_t rs=mk(ws, WS_END);
  const int th=w>>1, hd=w&1, kh=w&1, hh=l>>5, r=l&31, tt=2*t+th;
  const f32x16 zero={};
  {
    rsrc_t rx=mk(x, S*D*2);
    u32x4 raw[14];
    { const int row=16*w+(l>>2);
      #pragma unroll
      for(int i=0;i<14;i++) raw[i]=ld16(rx, row*896+(l&3)*16+i*64, t*64*896); }
    prog_set(smem,w,0); BARL();
    const char LAS* x0=pin(smem+A_X+(th*56+hh)*512+((r^(2*hh))*16));
    const char LAS* x1=pin(smem+A_X+(th*56+hh)*512+((r^(4+2*hh))*16));
    const int q=(l&15)>>2, p=l&3, cb=(l>>4)&1;
    const char LAS* kb=pin(smem+A_K+q*448+2*hh*112+hd*48+(cb^th)*32+(p&1)*16+(p>>1)*8);
    const char LAS* vb=pin(smem+A_V+q*448+2*hh*112+hd*56+cb*32+p*8);
    const char LAS* qb=pin(smem+A_X+(8*hh+q)*128+((hd^(q>>1))*64)+cb*32+(p&1)*16+(p>>1)*8);
    const u32 kmask=pin(((r>>2)^(th*4))==(hd?0:7)? 0u:0xffffffffu);
    const u32 vmask=pin(r>=28? 0u:0xffffffffu), vone=pin(r==28? 0x3f803f80u:0u);
    char LAS* xs=pin(smem+w*2048+l*16); const char LAS* xr_=pin(smem+(w^2)*2048+l*16);
    const u32 kvo=pin(WS_KVP+(((2*j+hd)*2+th)*32+t)*1024+l*16);
    f32x16 aK=pin(zero+1e-6f);
    { char LAS* xw=pin(smem+A_X+(th*56+p)*512+((((16*w+(l>>2))&31)^(2*p))*16));
      SB();
      float s1=0,s2=0,s1b=0,s2b=0;
      #pragma unroll
      for(int i=0;i<14;i++){
        s1=dot2(raw[i][0],0x3f803f80u,s1); s2=dot2(raw[i][0],raw[i][0],s2); s1b=dot2(raw[i][1],0x3f803f80u,s1b); s2b=dot2(raw[i][1],raw[i][1],s2b);
        s1=dot2(raw[i][2],0x3f803f80u,s1); s2=dot2(raw[i][2],raw[i][2],s2); s1b=dot2(raw[i][3],0x3f803f80u,s1b); s2b=dot2(raw[i][3],raw[i][3],s2b);
        SB(); }
      asm volatile("" :: "v"(cjunk));
      STAMP(1);
      const float m=quad_sum(s1+s1b)*(1.f/448);
      const float var=relu(quad_sum(s2+s2b)*(1.f/448)-m*m);
      const u32 rlo=pk(__builtin_amdgcn_rsqf(var+1e-5f),0.f), rhi=rlo<<16; const float sh=-m*lo(rlo);
      #pragma unroll
      for(int i=0;i<14;i++){ raw[i]=norm8(raw[i],rlo,rhi,sh,raw[i?i-1:1]); *(LAS u32x4*)(xw+i*2048)=raw[i]; SB(); }
    }
    prog_set(smem,w,1);
    STAMP(2);
    SPIN(min(prog2(smem,th*2)*2,prog2(smem,4)),2);
    STAMP(3);
    u32x4 xf[28], fw[84], k0, k1;
    #define XRD(s) xf[s]=*(const LAS u32x4*)((((s)&1)?x1:x0)+(s)*1024)
    #define WRD(n) { if((n)<56){ const char LAS* a_=((n)<28?kb:vb)+((n)%28)*1792; fw[n]=trfrag(a_,a_+112); } else { const char LAS* a_=qb+((n)-56)*2048; fw[n]=trfrag(a_,a_+512); } }
    #define NEED(v) { if((u32)__builtin_amdgcn_readfirstlane(pf)<(v)) SPIN(prog2(smem,4),v); asm volatile("" ::: "memory"); }
    f32x16 aV=zero; u32 pf=0; float ph[16];
    #define RDN(n) ((n)<28-PDK? (n)+PDK : ((n)<28-PDK+(PDV-PDK)? 2*(n)-(28-PDK)+PDK+1 : (n)+PDV))
    #pragma unroll
    for(int n=0;n<PDK;n++){ XRD(n); WRD(n) }
    SB();
    SFOR(n,0,48)
      SFOR(m,(n? RDN(n-1):PDK-1)+1,(RDN(n)<48? RDN(n)+1:48))
        if constexpr(m==13||m==29||m==37) pf=prog2(smem,4);
        if constexpr(m==16) NEED(4)
        if constexpr(m==32) NEED(5)
        if constexpr(m==40) NEED(6)
        if constexpr(m==28) prog_set(smem,w,2);
        if constexpr(m<28) XRD(m);
        WRD(m)
      SEND_
      if constexpr(n==12) STAMP(21);
      if constexpr(n==28) STAMP(22);
      if constexpr(n<28) aK=mfma(xf[n],fw[n],aK); else aV=mfma(xf[n-28],fw[n],aV);
      if constexpr(n>=30 && n<46){ constexpr int e=n-30; ph[e]=phie(aK[e]);
        if constexpr(e&1){ const u32 v=pk(ph[e-1],ph[e])&kmask; if constexpr(e<8) k0[e>>1]=v; else k1[(e>>1)-4]=v; } }
      SB();
    SEND_
    BAR(); STAMP(4);
    SFOR(n,48,55) WRD(n) SEND_
    SB();
    SFOR(n,48,56) if constexpr(n==48) WRD(55) aV=mfma(xf[n-28],fw[n],aV); SB(); SEND_
    { u32x4 v0=pack8(aV,0), v1=pack8(aV,1);
      #pragma unroll
      for(int e=0;e<4;e++){ v0[e]=(v0[e]&vmask)|vone; v1[e]=(v1[e]&vmask)|vone; }
      f32x16 kv=mfma(k0,v0,zero); kv=mfma(k1,v1,kv);
      *(LAS f32x4*)xs=qd(kv,2); *(LAS f32x4*)(xs+1024)=qd(kv,3);
      BARL();
      const f32x4 ka=qd(kv,0)+*(const LAS f32x4*)xr_, kc=qd(kv,1)+*(const LAS f32x4*)(xr_+1024);
      st16c(u32x4{pk(ka.x,ka.y),pk(ka.z,ka.w),pk(kc.x,kc.y),pk(kc.z,kc.w)}, rs, kvo);
    }
    STAMP(5);
    if(j<7){
      f32x16 aQ=zero+1e-6f;
      #pragma unroll
      for(int n=56;n<56+PDV;n++) WRD(n)
      SB();
      #pragma unroll
      for(int n=56;n<84;n++){
        if(n+PDV==69) pf=prog2(smem,4);
        if(n+PDV==72) NEED(8)
        if(n==56+B3POS){ BARV(); STAMP(6); }
        if(n+PDV<84) WRD(n+PDV)
        aQ=mfma(fw[n],xf[n-56],aQ); SB(); }
      #pragma unroll
      for(int g=0;g<2;g++) st16c(u32x4{pk(phie(aQ[8*g]),phie(aQ[8*g+1])),pk(phie(aQ[8*g+2]),phie(aQ[8*g+3])),pk(phie(aQ[8*g+4]),phie(aQ[8*g+5])),pk(phie(aQ[8*g+6]),phie(aQ[8*g+7]))},
                                  rs, WS_PQ+(tt*56+8*j+4*hd+2*g)*512+l*16);
      prog_set(smem,w,3);
      STAMP(7);
      WAITV(0); prog_set(smem,w,4);
    } else { BARV(); }
  }
  if(j==7){
    const u32 clo=pin(0x00003f80u), chi=pin(0x3f800000u), vo=pin((l>>4)*1024+(l&15)*16), so=pin(WS_KVT+t*1024+w*256+(l&15)*16+(l>>4)*4);
    STAMP(8);
    BAR();
    STAMP(9);
    { u32x4 v[8];
      #pragma unroll
      for(int i=0;i<8;i++) v[i]=ld16c(rs, vo, WS_KVP+t*32768+w*256+i*4096);
      SB();
      float a[8]={};
      #pragma unroll
      for(int i=0;i<8;i++){
        #pragma unroll
        for(int e=0;e<4;e++){ a[2*e]=dot2(v[i][e],clo,a[2*e]); a[2*e+1]=dot2(v[i][e],chi,a[2*e+1]); }
        SB(); }
      float bq[4];
      #pragma unroll
      for(int i=0;i<4;i++){ auto sw=__builtin_amdgcn_permlane32_swap(__float_as_uint(a[i]),__float_as_uint(a[i+4]),false,false); bq[i]=__uint_as_float(sw[0])+__uint_as_float(sw[1]); }
      float cq[2];
      #pragma unroll
      for(int i=0;i<2;i++){ auto sw=__builtin_amdgcn_permlane16_swap(__float_as_uint(bq[i]),__float_as_uint(bq[i+2]),false,false); cq[i]=__uint_as_float(sw[0])+__uint_as_float(sw[1]); }
      st4c(pk(cq[0],cq[1]), rs, so);
    }
    STAMP(10);
    BARV();
    STAMP(11);
    LAS float* red=(LAS float*)(smem+P1);
    u32 bb=0;
    if(t<28) bb=b1[t*64+hd*32+r];
    BAR();
    if(t<28){
      const char LAS* a=smem+img_rd(l,hd)+P0+th*14*2048;
      const u32x4 ones={0x3f803f80u,0x3f803f80u,0x3f803f80u,0x3f803f80u};
      u32x4 f[14];
      #pragma unroll
      for(int s=0;s<14;s++) f[s]=trfrag(a+s*2048,a+s*2048+1024);
      SB();
      f32x16 cs=zero;
      #pragma unroll
      for(int s=0;s<14;s++) cs=mfma(ones,f[s],cs);
      red[w*64+l]=cs[0];
    }
    BARL();
    if(t<28 && w<2){
      const float s=red[w*64+r]+red[(w+2)*64+r];
      u32 ch=pk(s,s); float rem=s-lo(ch); u32 cl=pk(rem,0.f);
      u32x4 o={ch,cl,bb|(bb<<16),0u}; if(hh) o=u32x4{0,0,0,0};
      st16c(o, rs, WS_AUG+(t*2+w)*1024+l*16);
    }
    BARV();
    STAMP(12);
    if(RWARM) warm_w2(w,28,4);
    STAMP(13);
    STAMP_FLUSH;
    return;
  }
  f32x16 hres;
  const char LAS* ik=smem+img_rd(l,kh); const char LAS* is=smem+img_rd(l,kh^1);
  {
    rsrc_t rb=mk(b2, D*2);
    u32x2 bv[4];
    #pragma unroll
    for(int gq=0;gq<4;gq++) bv[gq]=ld8(rb, hh*8+gq*16, j*128+kh*64);
    const char LAS* w0=pin(ik+P0+kh*8*28*128); const char LAS* w1_=pin(is+P0+kh*8*28*128);
    char LAS* es=pin(smem+9*SLOT+w*4096+l*16); const char LAS* er=pin(smem+9*SLOT+(w^1)*4096+l*16);
    const u32 ho=pin(WS_H+(tt*28+4*j+2*kh)*1024+l*16), sto=pin(hh? OOB : WS_ST+(tt*14+2*j+kh)*256+r*8), hm=pin(hh? 0u:0xffffffffu);
    SPINS(prog1(smem,6),1);
    STAMP(8);
    u32x4 pq[16], kv[16];
    #pragma unroll
    for(int i=0;i<16;i++) pq[i]=ld16c(rs, l*16+((i>>1)&1)*1536+(i&1)*1024, WS_PQ+(tt*56+kh*28+(i>>2)*7)*512);
    BAR();
    STAMP(9);
    #pragma unroll
    for(int i=0;i<16;i++) kv[i]=ld16c(rs, l*16+(i&3)*1024, WS_KVT+kh*16384+(i>>2)*4096);
    SB();
    f32x16 acc0, acc1=zero;
    #pragma unroll
    for(int gq=0;gq<4;gq++){ u32x2 xr=*(const LAS u32x2*)(smem+9*SLOT+w*4096+r*64+((gq^((r>>2)&3))*16)+hh*8);
      acc0[4*gq]=lo(xr[0]); acc0[4*gq+1]=hi(xr[0]); acc0[4*gq+2]=lo(xr[1]); acc0[4*gq+3]=hi(xr[1]); }
    acc0=pin(acc0);
    SB();
    f32x16 num[8]; u32x4 o0[8], o1[8];
    #define NUM(h) { num[h]=mfma(kv[2*(h)],pq[2*(h)],zero); num[h]=mfma(kv[2*(h)+1],pq[2*(h)+1],num[h]); }
    #define FIN(h) { const float rc=__builtin_amdgcn_rcpf(bcast_hi(num[h][12])+1e-6f); f32x16 n_=num[h]*rc; o0[h]=pack8(n_,0); o1[h]=pack8(n_,1); o1[h][2]&=hm; }
    NUM(0) NUM(1) SB(); FIN(0) SB();
    #pragma unroll
    for(int h=0;h<8;h++){
      const int ro=h*28*128;
      u32x4 fa=trfrag(w0+ro,w0+ro+1024), fb=trfrag(w1_+ro,w1_+ro+1024), fc_=trfrag(w0+ro+2048,w0+ro+3072), fd=trfrag(w1_+ro+2048,w1_+ro+3072);
      if(h+2<8) NUM(h+2)
      SB();
      acc1=mfma(fb,o0[h],acc1); acc1=mfma(fd,o1[h],acc1); acc0=mfma(fa,o0[h],acc0); acc0=mfma(fc_,o1[h],acc0);
      if(h+1<8) FIN(h+1)
      __builtin_amdgcn_sched_group_barrier(0x8,1,0); __builtin_amdgcn_sched_group_barrier(0x402,6,0);
      __builtin_amdgcn_sched_group_barrier(0x8,1,0); __builtin_amdgcn_sched_group_barrier(0x402,6,0);
      __builtin_amdgcn_sched_group_barrier(0x8,1,0); __builtin_amdgcn_sched_group_barrier(0x402,6,0);
      __builtin_amdgcn_sched_group_barrier(0x8,1,0); __builtin_amdgcn_sched_group_barrier(0x402,6,0);
      SB();
    }
    #pragma unroll
    for(int i=0;i<4;i++) *(LAS f32x4*)(es+i*1024)=qd(acc1,i);
    STAMP(10);
    BARL();
    #pragma unroll
    for(int i=0;i<4;i++){ const f32x4 v=qd(acc0,i)+*(const LAS f32x4*)(er+i*1024); acc0[4*i]=v.x; acc0[4*i+1]=v.y; acc0[4*i+2]=v.z; acc0[4*i+3]=v.w; }
    st16c(pack8(acc0,0), rs, ho); st16c(pack8(acc0,1), rs, ho+1024);
    SB();
    f32x2 s1=pr(acc0,0), s2=s1*s1;
    #pragma unroll
    for(int i=1;i<8;i++){ f32x2 va=pr(acc0,i); s1+=va; s2=__builtin_elementwise_fma(va,va,s2); }
    st8c(u32x2{__float_as_uint(xhalf_sum(s1.x+s1.y)),__float_as_uint(xhalf_sum(s2.x+s2.y))}, rs, sto);
    STAMP(11);
    SB();
    #pragma unroll
    for(int i=0;i<4;i++){ f32x2 va=pr(acc0,2*i)+un(bv[i][0]), vb=pr(acc0,2*i+1)+un(bv[i][1]);
      hres[4*i]=va.x; hres[4*i+1]=va.y; hres[4*i+2]=vb.x; hres[4*i+3]=vb.y; }
    hres=pin(hres);
    BARV();
  }
  auto stats=[&](const u32x2 (&st)[7], float& mu, float& var){
    f32x2 v=__builtin_bit_cast(f32x2,st[0]);
    #pragma unroll
    for(int i=1;i<7;i++) v+=__builtin_bit_cast(f32x2,st[i]);
    mu=xhalf_sum(v.x)*(1.f/448); var=relu(xhalf_sum(v.y)*(1.f/448)-mu*mu)+1e-5f; };
  {
    const int tf=4*cf+w;
    const char LAS* i0=smem+img_rd(l,0); const char LAS* i1=smem+img_rd(l,1);
    const char LAS* a0p=pin(i0+P1); const char LAS* a1p=pin(i1+P1); const char LAS* b0p=pin(i0+P0); const char LAS* b1p=pin(i1+P0);
    u32x4 fa[56], fb[56];
    #define RDA(s) { const char LAS* a_=((s)<28?a0p:b0p)+((s)%28)*2048; fa[s]=trfrag(a_,a_+1024); }
    #define RDB(s) { const char LAS* a_=((s)<28?a1p:b1p)+((s)%28)*2048; fb[s]=trfrag(a_,a_+1024); }
    RDA(0) RDB(0) RDA(1) RDB(1) RDA(2) RDB(2)
    BAR();
    STAMP(12);
    u32x4 hf_[28], aug[4]; u32x2 st[7];
    #define LDF(s) hf_[s]=ld16c(rs, l*16+((s)&3)*1024, WS_H+(tf*28+((s)&~3))*1024)
    #pragma unroll
    for(int s=0;s<FPD;s++) LDF(s);
    SB();
    f32x16 a0=zero, a1=zero;
    u32x4 bs;
    #pragma unroll
    for(int s=0;s<28;s++){
      if(s==25){ BAR(); }
      if(s+FPD<28) LDF(s+FPD);
      RDA(s+3)
      a0=mfma(fa[s],hf_[s],a0); SB();
      if(s>=5 && s<12) st[s-5]=ld8c(rs, r*8+hh*1792+(s-5)*256, WS_ST+tf*3584);
      if(s>=14 && s<18) aug[s-14]=ld16c(rs, hh? OOB : l*16+(s-14)*1024, WS_AUG+fc*4096);
      RDB(s+3)
      a1=mfma(fb[s],hf_[s],a1);
      if(s==22){ float mu,var; stats(st,mu,var);
        const float sg=__builtin_amdgcn_sqrtf(var); u32 mh=pk(-mu,-mu); float mr=-mu-lo(mh); u32 sh=pk(sg,sg); float sr=sg-lo(sh);
        bs=u32x4{pk(lo(mh),mr), mh, pk(lo(sh),sr), 0u}; }
      SB();
    }
    STAMP(13);
    a0=mfma(aug[0],bs,a0); a1=mfma(aug[1],bs,a1);
    f32x16 b0=mfma(aug[2],bs,zero), b1_=mfma(aug[3],bs,zero);
    SB();
    const u32 ho=WS_H1+(tf*112+fh*56+j*4)*1024+l*16;
    u32x4 pa;
    #pragma unroll
    for(int s=0;s<28;s++){
      if(s+3<28) RDA(28+s+3)
      b0=mfma(fa[28+s],hf_[s],b0);
      if(s<8){ const int e=2*s; pa[e&3]=sq2(pr(s<4?a0:a1,e&7)); }
      SB();
      if(s+3<28) RDB(28+s+3)
      b1_=mfma(fb[28+s],hf_[s],b1_);
      if(s<8){ const int e=2*s+1; pa[e&3]=sq2(pr(s<4?a0:a1,e&7)); if(s&1) st16c(pa, rs, ho+(s>>1)*1024); }
      SB();
      if(s==PUBA){ BARV(); }
    }
    STAMP(14);
    #pragma unroll
    for(int k=0;k<4;k++){ const f32x16& a=k<2?b0:b1_; const int e=(k&1)*4;
      st16c(u32x4{sq2(pr(a,e)),sq2(pr(a,e+1)),sq2(pr(a,e+2)),sq2(pr(a,e+3))}, rs, ho+28672+k*1024); }
    STAMP(15);
  }
  {
    const u32 hb=WS_H1+(tt*112+kh*56)*1024;
    u32x4 a[56], fa[56], fb[56]; u32x2 st[7];
    const char LAS* g0=pin(ik+kh*8192); const char LAS* g1=pin(is+kh*8192);
    const char LAS* g0h=pin(g0+4*SLOT); const char LAS* g1h=pin(g1+4*SLOT);
    const char LAS* g0x=pin(g0+8*SLOT); const char LAS* g1x=pin(g1+8*SLOT);
    constexpr int slot[14]={7,8,9,4,5,6,0,1,2,3,7,8,9,4};
    #define GADDR(bl,bh,bx,s) ((slot[(s)>>2]<4?(bl):(slot[(s)>>2]<8?(bh):(bx)))+(slot[(s)>>2]&3)*SLOT+((s)&3)*2048)
    #define GRA(s) fa[s]=trfrag(GADDR(g0,g0h,g0x,s),GADDR(g0,g0h,g0x,s)+1024)
    #define GRB(s) fb[s]=trfrag(GADDR(g1,g1h,g1x,s),GADDR(g1,g1h,g1x,s)+1024)
    #define LDH(s) a[s]=ld16c(rs, l*16+((s)&3)*1024, hb+((s)&~3)*1024)
    BAR();
    STAMP(16);
    #pragma unroll
    for(int s=0;s<GPD;s++) LDH(s);
    GRA(0); GRB(0); GRA(1); GRB(1); GRA(2); GRB(2);
    WAITV(GPD); BAR();
    SB();
    f32x16 acc0=zero, acc1=zero; float rv;
    #pragma unroll
    for(int s=0;s<56;s++){
      if(s==28-GPD){ STAMP(17); BAR(); STAMP(18); }
      if(s==37){ BAR(); }
      if(s+GPD<56) LDH(s+GPD);
      if(s+3<56) GRB(s+3);
      acc1=mfma(fb[s],a[s],acc1); SB();
      if(s>=38 && s<45) st[s-38]=ld8c(rs, r*8+hh*1792+(s-38)*256, WS_ST+tt*3584);
      if(s+3<56) GRA(s+3);
      acc0=mfma(fa[s],a[s],acc0);
      if(s==53){ float mu,var; stats(st,mu,var); rv=pin(__builtin_amdgcn_rcpf(var)); }
      SB();
    }
    STAMP(19);
    #pragma unroll
    for(int i=0;i<4;i++) *(LAS f32x4*)(smem+5*SLOT+w*4096+i*1024+l*16)=qd(acc1,i);
    BARL();
    u32 o[8];
    #pragma unroll
    for(int i=0;i<4;i++){ const f32x4 v=(qd(acc0,i)+*(const LAS f32x4*)(smem+5*SLOT+(w^1)*4096+i*1024+l*16))*rv+qd(hres,i);
      o[2*i]=pk(v.x,v.y); o[2*i+1]=pk(v.z,v.w); }
    rsrc_t ro_=mk(out, S*D*2);
    #pragma unroll
    for(int k=0;k<2;k++){
      auto r0=__builtin_amdgcn_permlane32_swap(o[4*k],o[4*k+2],false,false);
      auto r1=__builtin_amdgcn_permlane32_swap(o[4*k+1],o[4*k+3],false,false);
      st16(u32x4{r0[0],r1[0],r0[1],r1[1]}, ro_, (tt*32+r)*896+j*128+kh*64+hh*16+k*32);
    }
    STAMP(20); STAMP_CLK1
    STAMP_FLUSH;
  }
}

extern "C" void kernel_launch(void* const* d_in, const int* in_sizes, int n_in, void* d_out, int out_size, void* d_ws, size_t ws_size, hipStream_t stream) {
  (void)hipFuncSetAttribute((const void*)fused, hipFuncAttributeMaxDynamicSharedMemorySize, LDS_BYTES);
  fused<<<256,512,LDS_BYTES,stream>>>((const u16*)d_in[0],(const u16*)d_in[1],(const u16*)d_in[2],(const u16*)d_in[4],(const u16*)d_in[5],(const u16*)d_in[6],(const u16*)d_in[7],(char*)d_ws,(u16*)d_out);
}
```
